```python
import torch

def _softmax(x, axis=-1):
    return torch.softmax(x, dim=axis)
def _gelu(x):
    return torch.nn.functional.gelu(x, approximate="tanh")
def _silu(x):
    return torch.nn.functional.silu(x)
def _sigmoid(x):
    return torch.sigmoid(x)
def _layernorm(x, axis=-1, eps=1e-5):
    m = x.mean(dim=axis, keepdim=True)
    v = x.var(dim=axis, keepdim=True, unbiased=False)
    return (x - m) / torch.sqrt(v + eps)
def _scatter_add(tgt, idx, src, axis=0):
    return tgt.clone().index_add_(axis, idx, src)
def _segment_sum(src, seg, n):
    out = torch.zeros((n,) + src.shape[1:], dtype=src.dtype)
    return out.index_add_(0, seg, src)
def _pad(x, pw):
    flat = tuple(v for pair in reversed(pw) for v in pair)
    return torch.nn.functional.pad(x, flat)

kernel_name = 'sequential_preLN__mha_block__dense_activation_squared_relu__prompt'

def setup_inputs():

    _w = (lambda a: (lambda t: t.to(torch.bfloat16) if t.is_floating_point() else t)(torch.as_tensor(a)))
    def _A_setup():
        B, S, D, H = 1, 2048, 640, 32
        return {
            "x": _w((lambda *s: torch.randn(*s, dtype=torch.bfloat16) * 0.05)(B, S, D)),
            "wqkv": _w((lambda *s: torch.randn(*s, dtype=torch.bfloat16) * 0.05)(D, 3 * D)),
            "wo": _w((lambda *s: torch.randn(*s, dtype=torch.bfloat16) * 0.05)(D, D)),
        }
    _A_in = _A_setup()

    def _B_setup():
        B, S, D, F = 1, 2048, 640, 1728
        return {
            "x": _w((lambda *s: torch.randn(*s, dtype=torch.bfloat16) * 0.05)(B, S, D)),
            "w1": _w((lambda *s: torch.randn(*s, dtype=torch.bfloat16) * 0.05)(D, F)),
            "b1": _w((lambda *s: torch.randn(*s, dtype=torch.bfloat16) * 0.4)(F)),
            "w2": _w((lambda *s: torch.randn(*s, dtype=torch.bfloat16) * 0.05)(F, D)),
            "b2": _w((lambda *s: torch.randn(*s, dtype=torch.bfloat16) * 3.2)(D)),
        }
    _B_in = _B_setup()

    return {
        "A_x": _A_in["x"],
        "A_wqkv": _A_in["wqkv"],
        "A_wo": _A_in["wo"],
        "B_x": _B_in["x"],
        "B_w1": _B_in["w1"],
        "B_b1": _B_in["b1"],
        "B_w2": _B_in["w2"],
        "B_b2": _B_in["b2"],
    }

def reference(A_x, A_wqkv, A_wo, B_x, B_w1, B_b1, B_w2, B_b2):
    def _A_ref(x, wqkv, wo):
        B, S, D = x.shape
        H = 32
        Dh = D // H
        qkv = (lambda x, *d: x.permute(*d))((x @ wqkv).reshape(B, S, 3, H, Dh), 2, 0, 3, 1, 4)
        q, k, v = qkv[0], qkv[1], qkv[2]
        scores = torch.einsum("bhqd,bhkd->bhqk", q, k) * (Dh ** -0.5)
        p = _softmax(scores, axis=-1)
        o = torch.einsum("bhqk,bhkd->bhqd", p, v)
        return (lambda x, *d: x.permute(*d))(o, 0, 2, 1, 3).reshape(B, S, D) @ wo

    def _B_ref(x, w1, b1, w2, b2):
        h = torch.einsum("bsd,df->bsf", x, w1) + b1
        zero = torch.zeros_like(h)
        r = torch.where(h > zero, h, zero)
        h = r * r
        out = torch.einsum("bsf,fd->bsd", h, w2) + b2
        return out

    x = A_x
    h = x + _A_ref(_layernorm(x, axis=-1), A_wqkv, A_wo)
    h2 = h + _B_ref(_layernorm(h, axis=-1), B_w1, B_b1, B_w2, B_b2)
    return h2
```

```cpp
#include <hip/hip_runtime.h>
#include <hip/hip_bf16.h>
#include <stdint.h>

#define LAS __attribute__((address_space(3)))
typedef __bf16 bf16x8 __attribute__((ext_vector_type(8)));
typedef __bf16 bf16x2 __attribute__((ext_vector_type(2)));
typedef short s16x4 __attribute__((ext_vector_type(4)));
typedef short s16x8 __attribute__((ext_vector_type(8)));
typedef float f32x16 __attribute__((ext_vector_type(16)));
typedef float f32x4 __attribute__((ext_vector_type(4)));
typedef float f32x2 __attribute__((ext_vector_type(2)));
typedef unsigned u32x4 __attribute__((ext_vector_type(4)));
typedef unsigned u32x2 __attribute__((ext_vector_type(2)));

constexpr int S_ = 2048, D_ = 640, H_ = 32, F_ = 1728;
constexpr int QK_TILE = 1280;
constexpr int KV_TILE = 2688;
constexpr int V_OFF = 1280;
constexpr float QSCALE = 0.22360679774997896f * 1.4426950408889634f;
constexpr float LN_EPS = 1e-5f;

constexpr size_t WS_Q = 0;
constexpr size_t WS_KV = WS_Q + (size_t)H_ * 64 * QK_TILE;
constexpr size_t WS_O = WS_KV + (size_t)H_ * 64 * KV_TILE;
constexpr size_t WS_H = WS_O + (size_t)S_ * D_ * 2;
constexpr size_t WS_H1 = WS_H + (size_t)S_ * D_ * 2;
constexpr size_t WS_ST = WS_H1 + (size_t)S_ * F_ * 2;
constexpr size_t WS_C1 = WS_ST + (size_t)8 * S_ * 8;
constexpr size_t WS_END = WS_C1 + (size_t)F_ * 4;

__device__ __forceinline__ unsigned pk_bf16(float a, float b) {
    f32x2 v = {a, b};
    return __builtin_bit_cast(unsigned, __builtin_convertvector(v, bf16x2));
}
__device__ __forceinline__ float bf_lo(unsigned u) { return __uint_as_float(u << 16); }
__device__ __forceinline__ float bf_hi(unsigned u) { return __uint_as_float(u & 0xffff0000u); }
__device__ __forceinline__ float swap32_add(float v) {
    auto r = __builtin_amdgcn_permlane32_swap(__float_as_uint(v), __float_as_uint(v), false, false);
    return __uint_as_float(r[0]) + __uint_as_float(r[1]);
}
__device__ __forceinline__ float swap32_max(float v) {
    auto r = __builtin_amdgcn_permlane32_swap(__float_as_uint(v), __float_as_uint(v), false, false);
    return __builtin_fmaxf(__uint_as_float(r[0]), __uint_as_float(r[1]));
}
__device__ __forceinline__ void dma16(unsigned m0v, unsigned voff, __amdgpu_buffer_rsrc_t rsrc, unsigned soff) {
    asm volatile("s_mov_b32 m0, %0\n\ts_nop 0\n\tbuffer_load_dwordx4 %1, %2, %3 offen lds"
                 :: "s"(m0v), "v"(voff), "s"(rsrc), "s"(soff) : "memory");
}
__device__ __forceinline__ __amdgpu_buffer_rsrc_t mk_rsrc(const void* p, unsigned bytes) {
    return __builtin_amdgcn_make_buffer_rsrc((void*)p, 0, bytes, 0x00020000);
}
__device__ __forceinline__ bf16x8 lds_rd128(unsigned a) { return *(LAS const bf16x8*)a; }
__device__ __forceinline__ s16x4 lds_tr(unsigned a) { return __builtin_amdgcn_ds_read_tr16_b64_v4i16((LAS s16x4*)a); }
__device__ __forceinline__ bf16x8 cat8(s16x4 a, s16x4 b) {
    s16x8 r = {a[0], a[1], a[2], a[3], b[0], b[1], b[2], b[3]};
    return __builtin_bit_cast(bf16x8, r);
}
#define MFMA32(a, b, c) __builtin_amdgcn_mfma_f32_32x32x16_bf16(a, b, c, 0, 0, 0)
#define MFMA32K8(a, b, c) __builtin_amdgcn_mfma_f32_32x32x8bf16_1k(a, b, c, 0, 0, 0)
#define MFMA16(a, b, c) __builtin_amdgcn_mfma_f32_16x16x32_bf16(a, b, c, 0, 0, 0)
#define SBAR() __builtin_amdgcn_sched_barrier(0)
#define WAIT_VM(n) asm volatile("s_waitcnt vmcnt(" #n ")" ::: "memory")
#define BARRIER() asm volatile("s_barrier" ::: "memory")
#define LGKM0_BARRIER() asm volatile("s_waitcnt lgkmcnt(0)\n\ts_barrier" ::: "memory")

constexpr int T1_STAGE = 32768, T1_NS = 4, T1_RING = T1_STAGE * T1_NS;
constexpr int T1_AUX = T1_RING;
constexpr int T1_LDS = T1_RING + 2048;

struct T1Args {
    const void* A;
    const void* W;
    char* ws;
    const void* bias;
    const void* w1;
};

template <int MODE>
__global__ __launch_bounds__(512, 2) void gemm_t1(T1Args a) {
    __shared__ __attribute__((aligned(16))) char smem[T1_LDS];
    const unsigned lds0 = (unsigned)(uintptr_t)(LAS char*)smem;
    constexpr int N = MODE == 0 ? 1920 : F_;
    constexpr int LDB = N * 2;
    const int tid = threadIdx.x, lane = tid & 63;
    const int w = __builtin_amdgcn_readfirstlane(tid >> 6);
    const int xcd = blockIdx.x & 7, j = blockIdx.x >> 3;
    const int rb = (xcd >> 1) * 4 + (j & 3);
    const int cb = MODE == 0 ? (xcd & 1) * 8 + (j >> 2) : (xcd & 1) * 7 + (j >> 2);
    if (MODE == 0 && cb >= 15) {
        const int blk = (xcd >> 1) * 4 + (j & 3);
        if (blk < 14) {
            const int cc = tid & 15, rg = tid >> 4, col = blk * 128 + cc * 8;
            float s[8];
#pragma unroll
            for (int i = 0; i < 8; ++i) s[i] = 0.f;
            if (col < F_) {
                const char* p = (const char*)a.w1 + (size_t)rg * (F_ * 2) + col * 2;
#pragma unroll 4
                for (int r = 0; r < 20; ++r) {
                    const u32x4 v = *(const u32x4*)(p + (size_t)r * 32 * (F_ * 2));
#pragma unroll
                    for (int i = 0; i < 4; ++i) { s[2 * i] += bf_lo(v[i]); s[2 * i + 1] += bf_hi(v[i]); }
                }
            }
            LAS float* red = (LAS float*)lds0;
#pragma unroll
            for (int i = 0; i < 8; ++i) red[rg * 128 + cc * 8 + i] = s[i];
            __syncthreads();
            if (tid < 128 && blk * 128 + tid < F_) {
                float t = 0.f;
#pragma unroll
                for (int r = 0; r < 32; ++r) t += red[r * 128 + tid];
                ((float*)(a.ws + WS_C1))[blk * 128 + tid] = t;
            }
        }
        return;
    }
    if (MODE == 1 && (j >> 2) >= 7) return;
    const int m0 = rb * 128, n0 = cb * 128;

    if (w >= 4) {
        const int lw = w - 4;
        const __amdgpu_buffer_rsrc_t rA = mk_rsrc(a.A, S_ * D_ * 2), rB = mk_rsrc(a.W, D_ * N * 2);
        unsigned vA[2], vB[4];
#pragma unroll
        for (int p = 0; p < 2; ++p) vA[p] = (32 * lw + (lane >> 3)) * (D_ * 2) + (((lane & 7) ^ (4 * p + (lane >> 4))) * 16);
#pragma unroll
        for (int p = 0; p < 4; ++p) vB[p] = (16 * lw + (lane >> 4)) * LDB + (((lane & 15) ^ ((lane >> 4) << 2) ^ p) * 16);
        const unsigned sA0 = m0 * (D_ * 2), sB0 = n0 * 2;
        auto issue = [&](int c) {
            const unsigned slot = lds0 + (c & (T1_NS - 1)) * T1_STAGE + lw * 4096;
#pragma unroll
            for (int p = 0; p < 4; ++p) dma16(slot + p * 1024, vA[p & 1], rA, sA0 + p * 8 * (D_ * 2) + c * 128);
#pragma unroll
            for (int p = 0; p < 4; ++p) dma16(slot + 16384 + p * 1024, vB[p], rB, sB0 + (c * 64 + 4 * p) * LDB);
        };
        issue(0); issue(1); issue(2); issue(3);
        WAIT_VM(24); BARRIER();
#pragma unroll 1
        for (int c = 0; c < 6; ++c) { WAIT_VM(16); BARRIER(); issue(c + 4); }
        WAIT_VM(16); BARRIER();
        WAIT_VM(8); BARRIER();
        WAIT_VM(0); BARRIER();
        return;
    }

    const int wr = w >> 1, wc = w & 1, r32 = lane & 31, hi = lane >> 5;
    LAS f32x2* stat = (LAS f32x2*)(lds0 + T1_AUX);
    LAS f32x2* colv = (LAS f32x2*)(lds0 + T1_AUX + 1024);
    if (MODE == 1) {
        const int t2 = tid;
        if (t2 < 128) {
            const f32x2* st = (const f32x2*)(a.ws + WS_ST) + m0 + t2;
            float s1 = 0.f, s2 = 0.f;
#pragma unroll
            for (int i = 0; i < 8; ++i) { const f32x2 v = st[i * S_]; s1 += v[0]; s2 += v[1]; }
            const float mean = s1 * (1.f / D_);
            const float var = __builtin_fmaxf(s2 * (1.f / D_) - mean * mean, 0.f);
            stat[t2] = (f32x2){mean, __builtin_amdgcn_rsqf(var + LN_EPS)};
        } else {
            const int n = n0 + t2 - 128;
            float cs = 0.f, b = 0.f;
            if (n < F_) { cs = ((const float*)(a.ws + WS_C1))[n]; b = bf_lo(((const unsigned short*)a.bias)[n]); }
            colv[t2 - 128] = (f32x2){cs, b};
        }
    }
    unsigned aA[2][4], aB[2][2];
#pragma unroll
    for (int t = 0; t < 2; ++t)
#pragma unroll
        for (int s = 0; s < 4; ++s)
            aA[t][s] = lds0 + (64 * wr + 32 * (t ^ wc) + r32) * 128 + (((2 * s + hi) ^ ((r32 >> 1) & 7)) * 16);
    {
        const int g = lane >> 4, i = lane & 15, q = i >> 2, p = i & 3;
#pragma unroll
        for (int u = 0; u < 2; ++u)
#pragma unroll
            for (int jj = 0; jj < 2; ++jj) {
                const int k = 8 * (g >> 1) + 4 * jj + q, ch = 8 * wc + 4 * (u ^ wr) + 2 * (g & 1) + (p >> 1);
                const int swz = (q << 2) | (2 * (g >> 1) + jj);
                aB[u][jj] = lds0 + 16384 + k * 256 + ((ch ^ swz) * 16) + 8 * (p & 1);
            }
    }
    const bool vblk = (MODE == 0) && (cb >= 10);
    f32x16 acc[2][2];
#pragma unroll
    for (int t = 0; t < 2; ++t)
#pragma unroll
        for (int u = 0; u < 2; ++u) acc[t][u] = f32x16{};
    f32x2 st1 = {0.f, 0.f}, st2 = {0.f, 0.f}, cs1 = {0.f, 0.f};
    bf16x8 fa[2][2], fb[2][2];

#define T1_READ(buf, s) do { \
        fa[buf][0] = lds_rd128(aA[0][s]); fa[buf][1] = lds_rd128(aA[1][s]); \
        fb[buf][0] = cat8(lds_tr(aB[0][0] + 4096 * (s)), lds_tr(aB[0][1] + 4096 * (s))); \
        fb[buf][1] = cat8(lds_tr(aB[1][0] + 4096 * (s)), lds_tr(aB[1][1] + 4096 * (s))); } while (0)
#define T1_STATS(buf) do { if (MODE == 0) { \
        const u32x4 xa = __builtin_bit_cast(u32x4, fa[buf][0]), xb = __builtin_bit_cast(u32x4, fb[buf][0]); \
        _Pragma("unroll") for (int i = 0; i < 4; ++i) { \
            const f32x2 v = {bf_lo(xa[i]), bf_hi(xa[i])}; st1 += v; st2 += v * v; \
            const f32x2 z = {bf_lo(xb[i]), bf_hi(xb[i])}; cs1 += z; } } } while (0)
#define T1_MMA(buf) do { \
        if (vblk) { \
            acc[0][0] = MFMA32(fa[buf][0], fb[buf][0], acc[0][0]); acc[0][1] = MFMA32(fa[buf][0], fb[buf][1], acc[0][1]); \
            acc[1][0] = MFMA32(fa[buf][1], fb[buf][0], acc[1][0]); acc[1][1] = MFMA32(fa[buf][1], fb[buf][1], acc[1][1]); \
        } else { \
            acc[0][0] = MFMA32(fb[buf][0], fa[buf][0], acc[0][0]); acc[0][1] = MFMA32(fb[buf][1], fa[buf][0], acc[0][1]); \
            acc[1][0] = MFMA32(fb[buf][0], fa[buf][1], acc[1][0]); acc[1][1] = MFMA32(fb[buf][1], fa[buf][1], acc[1][1]); \
        } T1_STATS(buf); } while (0)

    BARRIER();
    T1_READ(0, 0);
#pragma unroll 1
    for (int c = 0; c < 10; ++c) {
        SBAR(); T1_READ(1, 1); SBAR(); T1_MMA(0);
        SBAR(); T1_READ(0, 2); SBAR(); T1_MMA(1);
        SBAR(); T1_READ(1, 3); SBAR(); T1_MMA(0);
        SBAR();
#pragma unroll
        for (int t = 0; t < 2; ++t)
#pragma unroll
            for (int s = 0; s < 4; ++s) aA[t][s] = lds0 + ((aA[t][s] - lds0 + T1_STAGE) & (T1_RING - 1));
#pragma unroll
        for (int u = 0; u < 2; ++u)
#pragma unroll
            for (int jj = 0; jj < 2; ++jj) aB[u][jj] = lds0 + ((aB[u][jj] - lds0 + T1_STAGE) & (T1_RING - 1));
        if (c < 9) { LGKM0_BARRIER(); T1_READ(0, 0); }
        SBAR(); T1_MMA(1);
    }
#undef T1_READ
#undef T1_MMA
#undef T1_STATS

    if (MODE == 0) {
        const float s1 = swap32_add(st1[0] + st1[1]), s2 = swap32_add(st2[0] + st2[1]), cs = swap32_add(cs1[0] + cs1[1]);
        const float mean = s1 * (1.f / D_);
        const float var = __builtin_fmaxf(s2 * (1.f / D_) - mean * mean, 0.f);
        if (hi == 0) {
            stat[64 * wr + 32 * wc + r32] = (f32x2){mean, __builtin_amdgcn_rsqf(var + LN_EPS)};
            colv[64 * wc + 32 * wr + r32] = (f32x2){cs, 0.f};
        }
    }
    LGKM0_BARRIER();

    if (!vblk) {
#pragma unroll
        for (int t = 0; t < 2; ++t) {
            const int ml = 64 * wr + 32 * (t ^ wc) + r32;
            const f32x2 ms = stat[ml];
#pragma unroll
            for (int u = 0; u < 2; ++u)
#pragma unroll
                for (int g = 0; g < 4; ++g) {
                    const int nl = 64 * wc + 32 * (u ^ wr) + 8 * g + 4 * hi;
                    const f32x4 c01 = *(LAS const f32x4*)(colv + nl), c23 = *(LAS const f32x4*)(colv + nl + 2);
                    float v[4];
                    v[0] = ms[1] * (acc[t][u][4 * g + 0] - ms[0] * c01[0]);
                    v[1] = ms[1] * (acc[t][u][4 * g + 1] - ms[0] * c01[2]);
                    v[2] = ms[1] * (acc[t][u][4 * g + 2] - ms[0] * c23[0]);
                    v[3] = ms[1] * (acc[t][u][4 * g + 3] - ms[0] * c23[2]);
                    if (MODE == 0) {
                        const float sc = cb < 5 ? QSCALE : 1.f;
                        const int nn = n0 + nl - (cb < 5 ? 0 : D_);
                        const int head = (nn * 3277) >> 16, d0 = nn - head * 20;
                        const int tok = m0 + ml;
                        char* base = cb < 5 ? a.ws + WS_Q + (size_t)(head * 64 + (tok >> 5)) * QK_TILE
                                            : a.ws + WS_KV + (size_t)(head * 64 + (tok >> 5)) * KV_TILE;
                        const int off = d0 < 16 ? (d0 >> 3) * 512 + r32 * 16 + (d0 & 4) * 2 : 1024 + r32 * 8;
                        *(u32x2*)(base + off) = (u32x2){pk_bf16(v[0] * sc, v[1] * sc), pk_bf16(v[2] * sc, v[3] * sc)};
                    } else {
                        v[0] = __builtin_fmaxf(v[0] + c01[1], 0.f); v[1] = __builtin_fmaxf(v[1] + c01[3], 0.f);
                        v[2] = __builtin_fmaxf(v[2] + c23[1], 0.f); v[3] = __builtin_fmaxf(v[3] + c23[3], 0.f);
                        if (n0 + nl < F_)
                            *(u32x2*)(a.ws + WS_H1 + (size_t)(m0 + ml) * (F_ * 2) + (n0 + nl) * 2) =
                                (u32x2){pk_bf16(v[0] * v[0], v[1] * v[1]), pk_bf16(v[2] * v[2], v[3] * v[3])};
                    }
                }
        }
    } else {
#pragma unroll
        for (int u = 0; u < 2; ++u) {
            const int nl = 64 * wc + 32 * (u ^ wr) + r32;
            const float cs = colv[nl][0];
            const int nn = n0 + nl - 2 * D_;
            const int head = (nn * 3277) >> 16, d = nn - head * 20;
#pragma unroll
            for (int t = 0; t < 2; ++t) {
                const int mt = 64 * wr + 32 * (t ^ wc);
                char* base = a.ws + WS_KV + (size_t)(head * 64 + ((m0 + mt) >> 5)) * KV_TILE + V_OFF + hi * 336;
#pragma unroll
                for (int s = 0; s < 2; ++s) {
                    u32x4 o;
#pragma unroll
                    for (int g = 0; g < 2; ++g) {
                        const int ml = mt + 8 * (2 * s + g) + 4 * hi;
                        const f32x4 s01 = *(LAS const f32x4*)(stat + ml), s23 = *(LAS const f32x4*)(stat + ml + 2);
                        const int r = 8 * s + 4 * g;
                        o[2 * g] = pk_bf16(s01[1] * (acc[t][u][r] - s01[0] * cs), s01[3] * (acc[t][u][r + 1] - s01[2] * cs));
                        o[2 * g + 1] = pk_bf16(s23[1] * (acc[t][u][r + 2] - s23[0] * cs), s23[3] * (acc[t][u][r + 3] - s23[2] * cs));
                    }
                    *(u32x4*)(base + s * 672 + d * 16) = o;
                    if (d == 0) *(u32x4*)(base + s * 672 + 320) = (u32x4){0x3f803f80u, 0x3f803f80u, 0x3f803f80u, 0x3f803f80u};
                }
            }
        }
    }
}

constexpr int AT_CHUNK = 8 * KV_TILE;
constexpr int AT_NS = 4;
constexpr int AT_LDS = AT_NS * AT_CHUNK + 64;

template <bool SAFE>
__device__ __forceinline__ void attn_stream(f32x16& o, unsigned lds0, __amdgpu_buffer_rsrc_t rKV, unsigned kvbase, int w, int lane,
                                            bf16x8 q0, s16x4 q1) {
    const int r32 = lane & 31, hi = lane >> 5;
    const unsigned vsrc = lane * 16;
    auto issue = [&](int c) {
        const unsigned slot = lds0 + (c & (AT_NS - 1)) * AT_CHUNK;
        const unsigned src = kvbase + c * AT_CHUNK;
        dma16(slot + w * 1024, vsrc, rKV, src + w * 1024);
        dma16(slot + (w + 8) * 1024, vsrc, rKV, src + (w + 8) * 1024);
        const int p = w < 5 ? w + 16 : w + 8;
        dma16(slot + p * 1024, vsrc, rKV, src + p * 1024);
    };
    issue(0); issue(1); issue(2);
    const unsigned oK0 = lane * 16, oK1 = 1024 + r32 * 8;
    const int vr = r32 <= 20 ? r32 : (r32 < 28 ? 20 : 4);
    const unsigned oV = V_OFF + hi * 336 + vr * 16;
    o = f32x16{};
    f32x16 negm = f32x16{};
    float m = 0.f;
    WAIT_VM(6); BARRIER();
    if (!SAFE) {
        f32x16 s = MFMA32(lds_rd128(lds0 + oK0), q0, f32x16{});
        s = MFMA32K8(*(LAS const s16x4*)(lds0 + oK1), q1, s);
        float mx = s[0];
#pragma unroll
        for (int i = 1; i < 16; ++i) mx = __builtin_fmaxf(mx, s[i]);
        mx = -swap32_max(mx);
#pragma unroll
        for (int i = 0; i < 16; ++i) negm[i] = mx;
    } else {
        m = -INFINITY;
    }
#pragma unroll 1
    for (int c = 0; c < 8; ++c) {
        if (c > 0) {
            if (c < 6) WAIT_VM(6); else if (c == 6) WAIT_VM(3); else WAIT_VM(0);
            BARRIER();
        }
        if (c + 3 < 8) issue(c + 3);
        const unsigned base = lds0 + (c & (AT_NS - 1)) * AT_CHUNK;
#pragma unroll
        for (int t = 0; t < 8; ++t) {
            const unsigned tb = base + t * KV_TILE;
            const bf16x8 k0 = lds_rd128(tb + oK0);
            const s16x4 k1 = *(LAS const s16x4*)(tb + oK1);
            const bf16x8 v0 = lds_rd128(tb + oV), v1 = lds_rd128(tb + oV + 672);
            f32x16 s = MFMA32(k0, q0, negm);
            s = MFMA32K8(k1, q1, s);
            if (SAFE) {
                s = MFMA32(k0, q0, f32x16{});
                s = MFMA32K8(k1, q1, s);
                float mx = s[0];
#pragma unroll
                for (int i = 1; i < 16; ++i) mx = __builtin_fmaxf(mx, s[i]);
                mx = __builtin_fmaxf(swap32_max(mx), m);
                const float alpha = __builtin_amdgcn_exp2f(m - mx);
                m = mx;
#pragma unroll
                for (int i = 0; i < 16; ++i) { s[i] -= m; o[i] *= alpha; }
            }
            u32x4 p0, p1;
#pragma unroll
            for (int i = 0; i < 4; ++i) {
                p0[i] = pk_bf16(__builtin_amdgcn_exp2f(s[2 * i]), __builtin_amdgcn_exp2f(s[2 * i + 1]));
                p1[i] = pk_bf16(__builtin_amdgcn_exp2f(s[8 + 2 * i]), __builtin_amdgcn_exp2f(s[8 + 2 * i + 1]));
            }
            o = MFMA32(v0, __builtin_bit_cast(bf16x8, p0), o);
            o = MFMA32(v1, __builtin_bit_cast(bf16x8, p1), o);
        }
        asm volatile("s_waitcnt lgkmcnt(0)" ::: "memory");
    }
}

__global__ __launch_bounds__(512, 2) void attn_kernel(char* ws) {
    __shared__ __attribute__((aligned(16))) char smem[AT_LDS];
    const unsigned lds0 = (unsigned)(uintptr_t)(LAS char*)smem;
    const int tid = threadIdx.x, lane = tid & 63, r32 = lane & 31, hi = lane >> 5;
    const int w = __builtin_amdgcn_readfirstlane(tid >> 6);
    const int xcd = blockIdx.x & 7, j = blockIdx.x >> 3;
    const int head = xcd * 4 + (j & 3), qb = j >> 2;
    const __amdgpu_buffer_rsrc_t rKV = mk_rsrc(ws + WS_KV, H_ * 64 * KV_TILE);
    const char* qp = ws + WS_Q + (size_t)(head * 64 + qb * 8 + w) * QK_TILE;
    const bf16x8 q0 = *(const bf16x8*)(qp + lane * 16);
    s16x4 q1 = {0, 0, 0, 0};
    if (hi == 0) q1 = *(const s16x4*)(qp + 1024 + r32 * 8);
    const unsigned kvbase = head * 64 * KV_TILE;
    f32x16 o;
    attn_stream<false>(o, lds0, rKV, kvbase, w, lane, q0, q1);
    float l = swap32_add(hi ? o[8] : 0.f);
    bool bad = !(l <= 1.0995116e12f);
#pragma unroll
    for (int i = 0; i < 12; ++i) bad = bad || !(__builtin_fabsf(o[i]) <= 3.0e38f);
    LAS int* flag = (LAS int*)(lds0 + AT_NS * AT_CHUNK);
    if (tid == 0) *flag = 0;
    LGKM0_BARRIER();
    if (__any(bad) && lane == 0) *flag = 1;
    LGKM0_BARRIER();
    if (*flag) {
        asm volatile("s_waitcnt lgkmcnt(0)\n\ts_barrier" ::: "memory");
        attn_stream<true>(o, lds0, rKV, kvbase, w, lane, q0, q1);
        l = swap32_add(hi ? o[8] : 0.f);
    }
    const float rl = 1.f / l;
    char* op = ws + WS_O + (size_t)(qb * 256 + w * 32 + r32) * (D_ * 2) + head * 40 + hi * 8;
    *(u32x2*)(op) = (u32x2){pk_bf16(o[0] * rl, o[1] * rl), pk_bf16(o[2] * rl, o[3] * rl)};
    *(u32x2*)(op + 16) = (u32x2){pk_bf16(o[4] * rl, o[5] * rl), pk_bf16(o[6] * rl, o[7] * rl)};
    if (hi == 0) *(u32x2*)(op + 32) = (u32x2){pk_bf16(o[8] * rl, o[9] * rl), pk_bf16(o[10] * rl, o[11] * rl)};
}

constexpr int T2_STAGE = 18432, T2_NS = 8, T2_LDS = T2_STAGE * T2_NS;

struct T2Args {
    const void* A;
    const void* W;
    const void* res;
    const void* bias;
    void* out;
    float* stats;
};

template <int MODE>
__global__ __launch_bounds__(512, 2) void gemm_t2(T2Args a) {
    __shared__ __attribute__((aligned(16))) char smem[T2_LDS];
    const unsigned lds0 = (unsigned)(uintptr_t)(LAS char*)smem;
    constexpr int K = MODE == 0 ? D_ : F_, NST = K / 64, LDA = K * 2, LDB = D_ * 2;
    const int tid = threadIdx.x, lane = tid & 63;
    const int w = __builtin_amdgcn_readfirstlane(tid >> 6);
    const int xcd = blockIdx.x & 7, j = blockIdx.x >> 3;
    const int rb = (xcd >> 1) * 8 + (j & 7), cb = (xcd & 1) * 4 + (j >> 3);
    const int m0 = rb * 64, n0 = cb * 80;

    if (w >= 4) {
        if (w == 7) return;
        const int lw = w - 4;
        const __amdgpu_buffer_rsrc_t rA = mk_rsrc(a.A, S_ * K * 2), rB = mk_rsrc(a.W, K * D_ * 2);
        unsigned voff[6];
#pragma unroll
        for (int i = 0; i < 6; ++i) {
            const int p = 6 * lw + i;
            if (p < 8) {
                voff[i] = (8 * p + (lane >> 3)) * LDA + (((lane & 7) ^ ((4 * p + (lane >> 4)) & 7)) * 16);
            } else {
                const int sl = 64 * (p - 8) + lane, lam = sl / 10, ch = sl - lam * 10;
                const int k = (lam & ~12) | ((lam & 4) << 1) | ((lam & 8) >> 1);
                voff[i] = k * LDB + ch * 16;
            }
        }
        const unsigned sA0 = m0 * LDA, sB0 = n0 * 2;
        auto issue = [&](int c) {
            const unsigned slot = lds0 + (c & (T2_NS - 1)) * T2_STAGE;
#pragma unroll
            for (int i = 0; i < 6; ++i) {
                const int p = 6 * lw + i;
                if (p < 8) dma16(slot + p * 1024, voff[i], rA, sA0 + c * 128);
                else       dma16(slot + p * 1024, voff[i], rB, sB0 + c * 64 * LDB);
            }
        };
#pragma unroll
        for (int c = 0; c < T2_NS; ++c) issue(c);
        WAIT_VM(42); BARRIER();
#pragma unroll 1
        for (int c = 0; c + T2_NS < NST; ++c) { WAIT_VM(36); BARRIER(); issue(c + T2_NS); }
        WAIT_VM(36); BARRIER();
        WAIT_VM(30); BARRIER();
        WAIT_VM(24); BARRIER();
        WAIT_VM(18); BARRIER();
        WAIT_VM(12); BARRIER();
        WAIT_VM(6); BARRIER();
        WAIT_VM(0); BARRIER();
        return;
    }

    const int rh = w >> 1, kg = w & 1, r16 = lane & 15, kq = lane >> 4;
    const int mrow = m0 + 32 * rh + 16 * kg + r16;
    u32x2 resv[5], biasv[5];
#pragma unroll
    for (int n = 0; n < 5; ++n) {
        resv[n] = *(const u32x2*)((const char*)a.res + (size_t)mrow * (D_ * 2) + (n0 + 16 * n + 4 * kq) * 2);
        if (MODE == 1) biasv[n] = *(const u32x2*)((const char*)a.bias + (n0 + 16 * n + 4 * kq) * 2);
    }
    unsigned aA[2], aB[2];
#pragma unroll
    for (int t = 0; t < 2; ++t)
        aA[t] = lds0 + (32 * rh + 16 * (t ^ kg) + r16) * 128 + (((4 * kg + kq) ^ ((r16 >> 1) & 7)) * 16);
    {
        const int i = lane & 15, q = i >> 2, p = i & 3;
#pragma unroll
        for (int jj = 0; jj < 2; ++jj) {
            const int k = 32 * kg + 8 * kq + 4 * jj + q;
            const int lam = (k & ~12) | ((k & 4) << 1) | ((k & 8) >> 1);
            aB[jj] = lds0 + 8192 + lam * 160 + p * 8;
        }
    }
    f32x4 acc[2][5];
#pragma unroll
    for (int t = 0; t < 2; ++t)
#pragma unroll
        for (int n = 0; n < 5; ++n) acc[t][n] = f32x4{};
    bf16x8 fa[2][2], fb[2][5];
#define T2_READ(buf, so) do { \
        fa[buf][0] = lds_rd128(aA[0] + (so)); fa[buf][1] = lds_rd128(aA[1] + (so)); \
        _Pragma("unroll") for (int n = 0; n < 5; ++n) fb[buf][n] = cat8(lds_tr(aB[0] + (so) + 32 * n), lds_tr(aB[1] + (so) + 32 * n)); } while (0)
#define T2_MMA(buf) do { _Pragma("unroll") for (int n = 0; n < 5; ++n) { \
        acc[0][n] = MFMA16(fb[buf][n], fa[buf][0], acc[0][n]); acc[1][n] = MFMA16(fb[buf][n], fa[buf][1], acc[1][n]); } } while (0)
    BARRIER();
    T2_READ(0, 0);
    unsigned so = 0;
#pragma unroll 1
    for (int c = 0; c < NST - 1; c += 2) {
        so = (so + T2_STAGE) % T2_LDS;
        LGKM0_BARRIER(); T2_READ(1, so); SBAR(); T2_MMA(0); SBAR();
        if (c + 2 < NST) {
            so = (so + T2_STAGE) % T2_LDS;
            LGKM0_BARRIER(); T2_READ(0, so); SBAR();
        }
        T2_MMA(1); SBAR();
    }
    if (NST & 1) T2_MMA(0);
#undef T2_READ
#undef T2_MMA
    LGKM0_BARRIER();
    LAS f32x4* xch = (LAS f32x4*)lds0;
#pragma unroll
    for (int n = 0; n < 5; ++n) xch[(w * 5 + n) * 64 + lane] = acc[1][n];
    LGKM0_BARRIER();
    float s1 = 0.f, s2 = 0.f;
#pragma unroll
    for (int n = 0; n < 5; ++n) {
        const f32x4 v = acc[0][n] + xch[((w ^ 1) * 5 + n) * 64 + lane];
        float r[4] = {v[0] + bf_lo(resv[n][0]), v[1] + bf_hi(resv[n][0]), v[2] + bf_lo(resv[n][1]), v[3] + bf_hi(resv[n][1])};
        if (MODE == 1) { r[0] += bf_lo(biasv[n][0]); r[1] += bf_hi(biasv[n][0]); r[2] += bf_lo(biasv[n][1]); r[3] += bf_hi(biasv[n][1]); }
        const u32x2 pk = {pk_bf16(r[0], r[1]), pk_bf16(r[2], r[3])};
        *(u32x2*)((char*)a.out + (size_t)mrow * (D_ * 2) + (n0 + 16 * n + 4 * kq) * 2) = pk;
        if (MODE == 0) {
            const float h0 = bf_lo(pk[0]), h1 = bf_hi(pk[0]), h2 = bf_lo(pk[1]), h3 = bf_hi(pk[1]);
            s1 += (h0 + h1) + (h2 + h3);
            s2 += (h0 * h0 + h1 * h1) + (h2 * h2 + h3 * h3);
        }
    }
    if (MODE == 0) {
        s1 += __shfl_xor(s1, 16); s2 += __shfl_xor(s2, 16);
        s1 += __shfl_xor(s1, 32); s2 += __shfl_xor(s2, 32);
        if (kq == 0) ((f32x2*)a.stats)[cb * S_ + mrow] = (f32x2){s1, s2};
    }
}

extern "C" void kernel_launch(void* const* d_in, const int* in_sizes, int n_in, void* d_out, int out_size, void* d_ws,
                              size_t ws_size, hipStream_t stream) {
    (void)in_sizes; (void)n_in; (void)out_size; (void)ws_size;
    char* ws = (char*)d_ws;
    T1Args q{d_in[0], d_in[1], ws, nullptr, d_in[4]};
    gemm_t1<0><<<256, 512, 0, stream>>>(q);
    attn_kernel<<<256, 512, 0, stream>>>(ws);
    T2Args o{ws + WS_O, d_in[2], d_in[0], nullptr, ws + WS_H, (float*)(ws + WS_ST)};
    gemm_t2<0><<<256, 512, 0, stream>>>(o);
    T1Args f{ws + WS_H, d_in[4], ws, d_in[5], nullptr};
    gemm_t1<1><<<256, 512, 0, stream>>>(f);
    T2Args g{ws + WS_H1, d_in[6], ws + WS_H, d_in[7], d_out, nullptr};
    gemm_t2<1><<<256, 512, 0, stream>>>(g);
}
```

```cpp
#include <hip/hip_runtime.h>
#include <hip/hip_bf16.h>
#include <stdint.h>

#define LAS __attribute__((address_space(3)))
typedef __bf16 bf16x8 __attribute__((ext_vector_type(8)));
typedef __bf16 bf16x2 __attribute__((ext_vector_type(2)));
typedef short s16x4 __attribute__((ext_vector_type(4)));
typedef short s16x8 __attribute__((ext_vector_type(8)));
typedef float f32x16 __attribute__((ext_vector_type(16)));
typedef float f32x4 __attribute__((ext_vector_type(4)));
typedef float f32x2 __attribute__((ext_vector_type(2)));
typedef unsigned u32x4 __attribute__((ext_vector_type(4)));
typedef unsigned u32x2 __attribute__((ext_vector_type(2)));
typedef unsigned long long u64;
typedef __amdgpu_buffer_rsrc_t rsrc_t;

constexpr int S_ = 2048, D_ = 640, H_ = 32, F_ = 1728;
constexpr int QK_TILE = 1280;
constexpr int KV_TILE = 2816, V_OFF = 1280;
__host__ __device__ constexpr int v_off(int row, int s, int hi) { return V_OFF + (row >> 2) * 256 + (((2 * s + hi) ^ ((row >> 2) & 3)) * 64) + (row & 3) * 16; }
constexpr float QSCALE = 0.22360679774997896f * 1.4426950408889634f;
constexpr float LN_EPS = 1e-5f;

constexpr unsigned WS_E = 0;
constexpr unsigned FL = 128;
constexpr unsigned WS_F0 = 4096;
constexpr unsigned WS_F1 = WS_F0 + 256 * FL;
constexpr unsigned WS_F2 = WS_F1 + 256 * FL;
constexpr unsigned WS_F3 = WS_F2 + 256 * FL;
constexpr unsigned WS_F4 = WS_F3 + 256 * FL;
constexpr unsigned WS_FW = WS_F4 + 256 * FL;
constexpr unsigned WS_XS = WS_FW + 64 * FL;
constexpr unsigned WS_CQ = WS_XS + S_ * 16;
constexpr unsigned WS_C1 = WS_CQ + 1920 * 16;
constexpr unsigned WS_ST = 294912;
constexpr unsigned WS_Q = WS_ST + 8 * S_ * 8 + 4096;
constexpr unsigned WS_KV = WS_Q + H_ * 64 * QK_TILE + 4096;
constexpr unsigned WS_O = WS_KV + H_ * 64 * KV_TILE + 4096;
constexpr unsigned WS_H = WS_O + S_ * D_ * 2 + 4096;
constexpr unsigned WS_H1 = WS_H + S_ * D_ * 2 + 4096;
constexpr unsigned WS_WT0 = WS_H1 + S_ * F_ * 2 + 4096;
constexpr unsigned WS_WT2 = WS_WT0 + 8 * 10 * 10240;
constexpr unsigned WS_END = WS_WT2 + 8 * 27 * 10240;

constexpr int LDS_TOTAL = 163840;
constexpr int L_AUX = 159744;
constexpr int L_SCR = L_AUX + 2048;
constexpr int T1_A = 0, T1_B = 65536, T1_NA = 4, T1_NB = 5;
constexpr int T2_NA = 6, T2_NB = 10, T2_A = 0, T2_B = T2_NA * 8192;

constexpr int AT_CHUNK = 8 * KV_TILE, AT_NS = 3, AT_RING = AT_NS * AT_CHUNK;

struct Args {
    const void* x; const void* wqkv; const void* wo; const void* w1; const void* b1; const void* w2; const void* b2;
    void* out; char* ws;
};

__device__ __forceinline__ unsigned pk_bf16(float a, float b) {
    f32x2 v = {a, b};
    return __builtin_bit_cast(unsigned, __builtin_convertvector(v, bf16x2));
}
__device__ __forceinline__ float bf_lo(unsigned u) { return __uint_as_float(u << 16); }
__device__ __forceinline__ float bf_hi(unsigned u) { return __uint_as_float(u & 0xffff0000u); }
__device__ __forceinline__ float bf_round(float v) { return bf_lo(pk_bf16(v, 0.f)); }
__device__ __forceinline__ f32x2 fma2(f32x2 a, f32x2 b, f32x2 c) { return __builtin_elementwise_fma(a, b, c); }
__device__ __forceinline__ f32x2 lo2(const f32x4& v) { return (f32x2){v[0], v[1]}; }
__device__ __forceinline__ f32x2 hi2(const f32x4& v) { return (f32x2){v[2], v[3]}; }
__device__ __forceinline__ unsigned pk_bf16(f32x2 v) { return __builtin_bit_cast(unsigned, __builtin_convertvector(v, bf16x2)); }
__device__ __forceinline__ float swap32_add(float v) {
    auto r = __builtin_amdgcn_permlane32_swap(__float_as_uint(v), __float_as_uint(v), false, false);
    return __uint_as_float(r[0]) + __uint_as_float(r[1]);
}
__device__ __forceinline__ float swap32_max(float v) {
    auto r = __builtin_amdgcn_permlane32_swap(__float_as_uint(v), __float_as_uint(v), false, false);
    return __builtin_fmaxf(__uint_as_float(r[0]), __uint_as_float(r[1]));
}
template <bool SC1>
__device__ __forceinline__ void dma16(unsigned m0v, unsigned voff, rsrc_t rsrc, unsigned soff) {
    if (SC1) asm volatile("s_mov_b32 m0, %0\n\ts_nop 0\n\tbuffer_load_dwordx4 %1, %2, %3 offen sc1 lds" :: "s"(m0v), "v"(voff), "s"(rsrc), "s"(soff) : "memory");
    else     asm volatile("s_mov_b32 m0, %0\n\ts_nop 0\n\tbuffer_load_dwordx4 %1, %2, %3 offen lds" :: "s"(m0v), "v"(voff), "s"(rsrc), "s"(soff) : "memory");
}
__device__ __forceinline__ rsrc_t mk_rsrc(const void* p, unsigned bytes) { return __builtin_amdgcn_make_buffer_rsrc((void*)p, 0, bytes, 0x00020000); }
__device__ __forceinline__ void st64(rsrc_t r, unsigned off, u32x2 v) { __builtin_amdgcn_raw_buffer_store_b64(v, r, off, 0, 16); }
__device__ __forceinline__ void st128(rsrc_t r, unsigned off, u32x4 v) { __builtin_amdgcn_raw_buffer_store_b128(v, r, off, 0, 16); }
__device__ __forceinline__ u32x2 ld64(rsrc_t r, unsigned off) { return __builtin_amdgcn_raw_buffer_load_b64(r, off, 0, 16); }
__device__ __forceinline__ u32x4 ld128(rsrc_t r, unsigned off) { return __builtin_amdgcn_raw_buffer_load_b128(r, off, 0, 16); }
__device__ __forceinline__ void touch(unsigned lds0, rsrc_t r, unsigned off) { dma16<false>(lds0 + L_SCR, off, r, 0u); }
struct Code { unsigned long long begin; unsigned len; };
__device__ __forceinline__ void code_touch(const Code& c, unsigned lds0, int lane) {
    touch(lds0, mk_rsrc((const void*)(c.begin & ~127ull), c.len & ~127u), ((blockIdx.x >> 3) * 64 + lane) * 128);
}
__device__ __forceinline__ bf16x8 lds_rd128(unsigned a) { return *(LAS const bf16x8*)a; }
#define MFMA32(a, b, c) __builtin_amdgcn_mfma_f32_32x32x16_bf16(a, b, c, 0, 0, 0)
#define MFMA32K8(a, b, c) __builtin_amdgcn_mfma_f32_32x32x8bf16_1k(a, b, c, 0, 0, 0)
#define WAIT_VM(n) asm volatile("s_waitcnt vmcnt(%0)" :: "i"(n) : "memory")
#define BARRIER() asm volatile("s_barrier" ::: "memory")
#define LGKM0_BARRIER() asm volatile("s_waitcnt lgkmcnt(0)\n\ts_barrier" ::: "memory")

struct Tag { unsigned lo, hi; };
__device__ __forceinline__ void wait_flags(rsrc_t ws, unsigned off, Tag t) {
    u64 t0 = 0;
    bool ok = false;
    for (unsigned i = 0;; ++i) {
        if (!ok) { const u32x2 v = ld64(ws, off); ok = v[0] == t.lo && v[1] == t.hi; }
        if (__all(ok)) break;
        if ((i & 63u) == 63u) {
            const u64 now = __builtin_amdgcn_s_memrealtime();
            if (t0 == 0) t0 = now;
            if (now - t0 > 2000000ull) break;
        }
    }
}
__device__ __forceinline__ void wait_flags3(rsrc_t ws, unsigned off0, unsigned off1, unsigned off2, Tag t) {
    u64 t0 = 0;
    bool ok = false;
    for (unsigned i = 0;; ++i) {
        if (!ok) {
            const u32x2 v = ld64(ws, off0), z = ld64(ws, off1), y = ld64(ws, off2);
            ok = v[0] == t.lo && v[1] == t.hi && z[0] == t.lo && z[1] == t.hi && y[0] == t.lo && y[1] == t.hi;
        }
        if (__all(ok)) break;
        if ((i & 63u) == 63u) {
            const u64 now = __builtin_amdgcn_s_memrealtime();
            if (t0 == 0) t0 = now;
            if (now - t0 > 2000000ull) break;
        }
    }
}
__device__ __forceinline__ f32x2 wait_granule(rsrc_t ws, unsigned off, Tag t, u32x4 v) {
    const u64 t0 = __builtin_amdgcn_s_memrealtime();
    while (!__all(v[2] == t.lo && v[3] == t.hi)) {
        if (__builtin_amdgcn_s_memrealtime() - t0 > 2000000ull) break;
        __builtin_amdgcn_s_sleep(1);
        v = ld128(ws, off);
    }
    return (f32x2){__uint_as_float(v[0]), __uint_as_float(v[1])};
}
__device__ __forceinline__ void set_flag(rsrc_t ws, unsigned off, Tag t) { st64(ws, off, (u32x2){t.lo, t.hi}); }

template <int NST, int N, int P, int LAG, int C, class F, class H>
__device__ __forceinline__ void ring_steps(F&& issue, H&& hook) {
    if constexpr (C < NST) {
        constexpr int y = C - 2 - LAG + N > N - 1 ? C - 2 - LAG + N : N - 1, last = y < NST - 1 ? y : NST - 1;
        WAIT_VM((last - C) * P);
        BARRIER();
        hook(C);
        if constexpr (C - 1 - LAG >= 0 && C - 1 - LAG + N < NST) issue(C - 1 - LAG + N);
        ring_steps<NST, N, P, LAG, C + 1>(issue, hook);
    }
}
__device__ __forceinline__ Tag read_tag(const char* ws) {
    u64 e;
    asm volatile("s_load_dwordx2 %0, %1, 0x0\n\ts_waitcnt lgkmcnt(0)" : "=s"(e) : "s"(ws + WS_E) : "memory");
    e += 1;
    Tag t; t.lo = (unsigned)e; t.hi = (unsigned)(e >> 32);
    return t;
}

#define T1_LOOP_ASM_QK \
    "s_barrier\n\t" \
    "ds_read_b128 v[128:131], v160 offset:0\n\t" \
    "ds_read_b64_tr_b16 v[136:137], v164 offset:0\n\t" \
    "ds_read_b64_tr_b16 v[138:139], v165 offset:0\n\t" \
    "ds_read_b64_tr_b16 v[140:141], v166 offset:0\n\t" \
    "ds_read_b64_tr_b16 v[142:143], v167 offset:0\n\t" \
    "ds_read_b128 v[132:135], v160 offset:4096\n\t" \
    "s_waitcnt lgkmcnt(3)\n\t" \
    "v_mfma_f32_32x32x16_bf16 v[64:79], v[136:139], v[128:131], v[64:79]\n\t" \
    "ds_read_b128 v[144:147], v161 offset:0\n\t" \
    "ds_read_b64_tr_b16 v[152:153], v164 offset:4096\n\t" \
    "ds_read_b64_tr_b16 v[154:155], v165 offset:4096\n\t" \
    "s_waitcnt lgkmcnt(4)\n\t" \
    "v_mfma_f32_32x32x16_bf16 v[80:95], v[140:143], v[128:131], v[80:95]\n\t" \
    "ds_read_b64_tr_b16 v[156:157], v166 offset:4096\n\t" \
    "ds_read_b64_tr_b16 v[158:159], v167 offset:4096\n\t" \
    "ds_read_b128 v[148:151], v161 offset:4096\n\t" \
    "s_waitcnt lgkmcnt(6)\n\t" \
    "v_mfma_f32_32x32x16_bf16 v[96:111], v[136:139], v[132:135], v[96:111]\n\t" \
    "v_mfma_f32_32x32x16_bf16 v[112:127], v[140:143], v[132:135], v[112:127]\n\t" \
    "s_waitcnt lgkmcnt(3)\n\t" \
    "v_mfma_f32_32x32x16_bf16 v[64:79], v[152:155], v[144:147], v[64:79]\n\t" \
    "ds_read_b128 v[128:131], v162 offset:0\n\t" \
    "ds_read_b64_tr_b16 v[136:137], v164 offset:8192\n\t" \
    "ds_read_b64_tr_b16 v[138:139], v165 offset:8192\n\t" \
    "s_waitcnt lgkmcnt(4)\n\t" \
    "v_mfma_f32_32x32x16_bf16 v[80:95], v[156:159], v[144:147], v[80:95]\n\t" \
    "ds_read_b64_tr_b16 v[140:141], v166 offset:8192\n\t" \
    "ds_read_b64_tr_b16 v[142:143], v167 offset:8192\n\t" \
    "ds_read_b128 v[132:135], v162 offset:4096\n\t" \
    "s_waitcnt lgkmcnt(6)\n\t" \
    "v_mfma_f32_32x32x16_bf16 v[96:111], v[152:155], v[148:151], v[96:111]\n\t" \
    "v_mfma_f32_32x32x16_bf16 v[112:127], v[156:159], v[148:151], v[112:127]\n\t" \
    "s_waitcnt lgkmcnt(3)\n\t" \
    "v_mfma_f32_32x32x16_bf16 v[64:79], v[136:139], v[128:131], v[64:79]\n\t" \
    "ds_read_b128 v[144:147], v163 offset:0\n\t" \
    "ds_read_b64_tr_b16 v[152:153], v164 offset:12288\n\t" \
    "ds_read_b64_tr_b16 v[154:155], v165 offset:12288\n\t" \
    "s_waitcnt lgkmcnt(4)\n\t" \
    "v_mfma_f32_32x32x16_bf16 v[80:95], v[140:143], v[128:131], v[80:95]\n\t" \
    "ds_read_b64_tr_b16 v[156:157], v166 offset:12288\n\t" \
    "ds_read_b64_tr_b16 v[158:159], v167 offset:12288\n\t" \
    "ds_read_b128 v[148:151], v163 offset:4096\n\t" \
    "s_waitcnt lgkmcnt(6)\n\t" \
    "v_mfma_f32_32x32x16_bf16 v[96:111], v[136:139], v[132:135], v[96:111]\n\t" \
    "v_mfma_f32_32x32x16_bf16 v[112:127], v[140:143], v[132:135], v[112:127]\n\t" \
    "s_waitcnt lgkmcnt(3)\n\t" \
    "v_mfma_f32_32x32x16_bf16 v[64:79], v[152:155], v[144:147], v[64:79]\n\t" \
    "s_waitcnt lgkmcnt(0)\n\t" \
    "s_barrier\n\t" \
    "ds_read_b128 v[128:131], v160 offset:16384\n\t" \
    "ds_read_b64_tr_b16 v[136:137], v164 offset:16384\n\t" \
    "ds_read_b64_tr_b16 v[138:139], v165 offset:16384\n\t" \
    "v_mfma_f32_32x32x16_bf16 v[80:95], v[156:159], v[144:147], v[80:95]\n\t" \
    "ds_read_b64_tr_b16 v[140:141], v166 offset:16384\n\t" \
    "ds_read_b64_tr_b16 v[142:143], v167 offset:16384\n\t" \
    "ds_read_b128 v[132:135], v160 offset:20480\n\t" \
    "v_mfma_f32_32x32x16_bf16 v[96:111], v[152:155], v[148:151], v[96:111]\n\t" \
    "v_mfma_f32_32x32x16_bf16 v[112:127], v[156:159], v[148:151], v[112:127]\n\t" \
    "s_waitcnt lgkmcnt(3)\n\t" \
    "v_mfma_f32_32x32x16_bf16 v[64:79], v[136:139], v[128:131], v[64:79]\n\t" \
    "ds_read_b128 v[144:147], v161 offset:16384\n\t" \
    "ds_read_b64_tr_b16 v[152:153], v164 offset:20480\n\t" \
    "ds_read_b64_tr_b16 v[154:155], v165 offset:20480\n\t" \
    "s_waitcnt lgkmcnt(4)\n\t" \
    "v_mfma_f32_32x32x16_bf16 v[80:95], v[140:143], v[128:131], v[80:95]\n\t" \
    "ds_read_b64_tr_b16 v[156:157], v166 offset:20480\n\t" \
    "ds_read_b64_tr_b16 v[158:159], v167 offset:20480\n\t" \
    "ds_read_b128 v[148:151], v161 offset:20480\n\t" \
    "s_waitcnt lgkmcnt(6)\n\t" \
    "v_mfma_f32_32x32x16_bf16 v[96:111], v[136:139], v[132:135], v[96:111]\n\t" \
    "v_mfma_f32_32x32x16_bf16 v[112:127], v[140:143], v[132:135], v[112:127]\n\t" \
    "s_waitcnt lgkmcnt(3)\n\t" \
    "v_mfma_f32_32x32x16_bf16 v[64:79], v[152:155], v[144:147], v[64:79]\n\t" \
    "ds_read_b128 v[128:131], v162 offset:16384\n\t" \
    "ds_read_b64_tr_b16 v[136:137], v164 offset:24576\n\t" \
    "ds_read_b64_tr_b16 v[138:139], v165 offset:24576\n\t" \
    "s_waitcnt lgkmcnt(4)\n\t" \
    "v_mfma_f32_32x32x16_bf16 v[80:95], v[156:159], v[144:147], v[80:95]\n\t" \
    "ds_read_b64_tr_b16 v[140:141], v166 offset:24576\n\t" \
    "ds_read_b64_tr_b16 v[142:143], v167 offset:24576\n\t" \
    "ds_read_b128 v[132:135], v162 offset:20480\n\t" \
    "s_waitcnt lgkmcnt(6)\n\t" \
    "v_mfma_f32_32x32x16_bf16 v[96:111], v[152:155], v[148:151], v[96:111]\n\t" \
    "v_mfma_f32_32x32x16_bf16 v[112:127], v[156:159], v[148:151], v[112:127]\n\t" \
    "s_waitcnt lgkmcnt(3)\n\t" \
    "v_mfma_f32_32x32x16_bf16 v[64:79], v[136:139], v[128:131], v[64:79]\n\t" \
    "ds_read_b128 v[144:147], v163 offset:16384\n\t" \
    "ds_read_b64_tr_b16 v[152:153], v164 offset:28672\n\t" \
    "ds_read_b64_tr_b16 v[154:155], v165 offset:28672\n\t" \
    "s_waitcnt lgkmcnt(4)\n\t" \
    "v_mfma_f32_32x32x16_bf16 v[80:95], v[140:143], v[128:131], v[80:95]\n\t" \
    "ds_read_b64_tr_b16 v[156:157], v166 offset:28672\n\t" \
    "ds_read_b64_tr_b16 v[158:159], v167 offset:28672\n\t" \
    "ds_read_b128 v[148:151], v163 offset:20480\n\t" \
    "s_waitcnt lgkmcnt(6)\n\t" \
    "v_mfma_f32_32x32x16_bf16 v[96:111], v[136:139], v[132:135], v[96:111]\n\t" \
    "v_mfma_f32_32x32x16_bf16 v[112:127], v[140:143], v[132:135], v[112:127]\n\t" \
    "s_waitcnt lgkmcnt(3)\n\t" \
    "v_mfma_f32_32x32x16_bf16 v[64:79], v[152:155], v[144:147], v[64:79]\n\t" \
    "s_waitcnt lgkmcnt(0)\n\t" \
    "s_barrier\n\t" \
    "ds_read_b128 v[128:131], v160 offset:32768\n\t" \
    "ds_read_b64_tr_b16 v[136:137], v164 offset:32768\n\t" \
    "ds_read_b64_tr_b16 v[138:139], v165 offset:32768\n\t" \
    "v_mfma_f32_32x32x16_bf16 v[80:95], v[156:159], v[144:147], v[80:95]\n\t" \
    "ds_read_b64_tr_b16 v[140:141], v166 offset:32768\n\t" \
    "ds_read_b64_tr_b16 v[142:143], v167 offset:32768\n\t" \
    "ds_read_b128 v[132:135], v160 offset:36864\n\t" \
    "v_mfma_f32_32x32x16_bf16 v[96:111], v[152:155], v[148:151], v[96:111]\n\t" \
    "v_mfma_f32_32x32x16_bf16 v[112:127], v[156:159], v[148:151], v[112:127]\n\t" \
    "s_waitcnt lgkmcnt(3)\n\t" \
    "v_mfma_f32_32x32x16_bf16 v[64:79], v[136:139], v[128:131], v[64:79]\n\t" \
    "ds_read_b128 v[144:147], v161 offset:32768\n\t" \
    "ds_read_b64_tr_b16 v[152:153], v164 offset:36864\n\t" \
    "ds_read_b64_tr_b16 v[154:155], v165 offset:36864\n\t" \
    "s_waitcnt lgkmcnt(4)\n\t" \
    "v_mfma_f32_32x32x16_bf16 v[80:95], v[140:143], v[128:131], v[80:95]\n\t" \
    "ds_read_b64_tr_b16 v[156:157], v166 offset:36864\n\t" \
    "ds_read_b64_tr_b16 v[158:159], v167 offset:36864\n\t" \
    "ds_read_b128 v[148:151], v161 offset:36864\n\t" \
    "s_waitcnt lgkmcnt(6)\n\t" \
    "v_mfma_f32_32x32x16_bf16 v[96:111], v[136:139], v[132:135], v[96:111]\n\t" \
    "v_mfma_f32_32x32x16_bf16 v[112:127], v[140:143], v[132:135], v[112:127]\n\t" \
    "s_waitcnt lgkmcnt(3)\n\t" \
    "v_mfma_f32_32x32x16_bf16 v[64:79], v[152:155], v[144:147], v[64:79]\n\t" \
    "ds_read_b128 v[128:131], v162 offset:32768\n\t" \
    "ds_read_b64_tr_b16 v[136:137], v164 offset:40960\n\t" \
    "ds_read_b64_tr_b16 v[138:139], v165 offset:40960\n\t" \
    "s_waitcnt lgkmcnt(4)\n\t" \
    "v_mfma_f32_32x32x16_bf16 v[80:95], v[156:159], v[144:147], v[80:95]\n\t" \
    "ds_read_b64_tr_b16 v[140:141], v166 offset:40960\n\t" \
    "ds_read_b64_tr_b16 v[142:143], v167 offset:40960\n\t" \
    "ds_read_b128 v[132:135], v162 offset:36864\n\t" \
    "s_waitcnt lgkmcnt(6)\n\t" \
    "v_mfma_f32_32x32x16_bf16 v[96:111], v[152:155], v[148:151], v[96:111]\n\t" \
    "v_mfma_f32_32x32x16_bf16 v[112:127], v[156:159], v[148:151], v[112:127]\n\t" \
    "s_waitcnt lgkmcnt(3)\n\t" \
    "v_mfma_f32_32x32x16_bf16 v[64:79], v[136:139], v[128:131], v[64:79]\n\t" \
    "ds_read_b128 v[144:147], v163 offset:32768\n\t" \
    "ds_read_b64_tr_b16 v[152:153], v164 offset:45056\n\t" \
    "ds_read_b64_tr_b16 v[154:155], v165 offset:45056\n\t" \
    "s_waitcnt lgkmcnt(4)\n\t" \
    "v_mfma_f32_32x32x16_bf16 v[80:95], v[140:143], v[128:131], v[80:95]\n\t" \
    "ds_read_b64_tr_b16 v[156:157], v166 offset:45056\n\t" \
    "ds_read_b64_tr_b16 v[158:159], v167 offset:45056\n\t" \
    "ds_read_b128 v[148:151], v163 offset:36864\n\t" \
    "s_waitcnt lgkmcnt(6)\n\t" \
    "v_mfma_f32_32x32x16_bf16 v[96:111], v[136:139], v[132:135], v[96:111]\n\t" \
    "v_mfma_f32_32x32x16_bf16 v[112:127], v[140:143], v[132:135], v[112:127]\n\t" \
    "s_waitcnt lgkmcnt(3)\n\t" \
    "v_mfma_f32_32x32x16_bf16 v[64:79], v[152:155], v[144:147], v[64:79]\n\t" \
    "s_waitcnt lgkmcnt(0)\n\t" \
    "s_barrier\n\t" \
    "ds_read_b128 v[128:131], v160 offset:49152\n\t" \
    "ds_read_b64_tr_b16 v[136:137], v164 offset:49152\n\t" \
    "ds_read_b64_tr_b16 v[138:139], v165 offset:49152\n\t" \
    "v_mfma_f32_32x32x16_bf16 v[80:95], v[156:159], v[144:147], v[80:95]\n\t" \
    "ds_read_b64_tr_b16 v[140:141], v166 offset:49152\n\t" \
    "ds_read_b64_tr_b16 v[142:143], v167 offset:49152\n\t" \
    "ds_read_b128 v[132:135], v160 offset:53248\n\t" \
    "v_mfma_f32_32x32x16_bf16 v[96:111], v[152:155], v[148:151], v[96:111]\n\t" \
    "v_mfma_f32_32x32x16_bf16 v[112:127], v[156:159], v[148:151], v[112:127]\n\t" \
    "s_waitcnt lgkmcnt(3)\n\t" \
    "v_mfma_f32_32x32x16_bf16 v[64:79], v[136:139], v[128:131], v[64:79]\n\t" \
    "ds_read_b128 v[144:147], v161 offset:49152\n\t" \
    "ds_read_b64_tr_b16 v[152:153], v164 offset:53248\n\t" \
    "ds_read_b64_tr_b16 v[154:155], v165 offset:53248\n\t" \
    "s_waitcnt lgkmcnt(4)\n\t" \
    "v_mfma_f32_32x32x16_bf16 v[80:95], v[140:143], v[128:131], v[80:95]\n\t" \
    "ds_read_b64_tr_b16 v[156:157], v166 offset:53248\n\t" \
    "ds_read_b64_tr_b16 v[158:159], v167 offset:53248\n\t" \
    "ds_read_b128 v[148:151], v161 offset:53248\n\t" \
    "s_waitcnt lgkmcnt(6)\n\t" \
    "v_mfma_f32_32x32x16_bf16 v[96:111], v[136:139], v[132:135], v[96:111]\n\t" \
    "v_mfma_f32_32x32x16_bf16 v[112:127], v[140:143], v[132:135], v[112:127]\n\t" \
    "s_waitcnt lgkmcnt(3)\n\t" \
    "v_mfma_f32_32x32x16_bf16 v[64:79], v[152:155], v[144:147], v[64:79]\n\t" \
    "ds_read_b128 v[128:131], v162 offset:49152\n\t" \
    "ds_read_b64_tr_b16 v[136:137], v164 offset:57344\n\t" \
    "ds_read_b64_tr_b16 v[138:139], v165 offset:57344\n\t" \
    "s_waitcnt lgkmcnt(4)\n\t" \
    "v_mfma_f32_32x32x16_bf16 v[80:95], v[156:159], v[144:147], v[80:95]\n\t" \
    "ds_read_b64_tr_b16 v[140:141], v166 offset:57344\n\t" \
    "ds_read_b64_tr_b16 v[142:143], v167 offset:57344\n\t" \
    "ds_read_b128 v[132:135], v162 offset:53248\n\t" \
    "s_waitcnt lgkmcnt(6)\n\t" \
    "v_mfma_f32_32x32x16_bf16 v[96:111], v[152:155], v[148:151], v[96:111]\n\t" \
    "v_mfma_f32_32x32x16_bf16 v[112:127], v[156:159], v[148:151], v[112:127]\n\t" \
    "s_waitcnt lgkmcnt(3)\n\t" \
    "v_mfma_f32_32x32x16_bf16 v[64:79], v[136:139], v[128:131], v[64:79]\n\t" \
    "ds_read_b128 v[144:147], v163 offset:49152\n\t" \
    "ds_read_b64_tr_b16 v[152:153], v164 offset:61440\n\t" \
    "ds_read_b64_tr_b16 v[154:155], v165 offset:61440\n\t" \
    "s_waitcnt lgkmcnt(4)\n\t" \
    "v_mfma_f32_32x32x16_bf16 v[80:95], v[140:143], v[128:131], v[80:95]\n\t" \
    "ds_read_b64_tr_b16 v[156:157], v166 offset:61440\n\t" \
    "ds_read_b64_tr_b16 v[158:159], v167 offset:61440\n\t" \
    "ds_read_b128 v[148:151], v163 offset:53248\n\t" \
    "s_waitcnt lgkmcnt(6)\n\t" \
    "v_mfma_f32_32x32x16_bf16 v[96:111], v[136:139], v[132:135], v[96:111]\n\t" \
    "v_mfma_f32_32x32x16_bf16 v[112:127], v[140:143], v[132:135], v[112:127]\n\t" \
    "s_waitcnt lgkmcnt(3)\n\t" \
    "v_mfma_f32_32x32x16_bf16 v[64:79], v[152:155], v[144:147], v[64:79]\n\t" \
    "s_waitcnt lgkmcnt(0)\n\t" \
    "s_barrier\n\t" \
    "ds_read_b128 v[128:131], v160 offset:0\n\t" \
    "ds_read_b64_tr_b16 v[136:137], v168 offset:0\n\t" \
    "ds_read_b64_tr_b16 v[138:139], v169 offset:0\n\t" \
    "v_mfma_f32_32x32x16_bf16 v[80:95], v[156:159], v[144:147], v[80:95]\n\t" \
    "ds_read_b64_tr_b16 v[140:141], v170 offset:0\n\t" \
    "ds_read_b64_tr_b16 v[142:143], v171 offset:0\n\t" \
    "ds_read_b128 v[132:135], v160 offset:4096\n\t" \
    "v_mfma_f32_32x32x16_bf16 v[96:111], v[152:155], v[148:151], v[96:111]\n\t" \
    "v_mfma_f32_32x32x16_bf16 v[112:127], v[156:159], v[148:151], v[112:127]\n\t" \
    "s_waitcnt lgkmcnt(3)\n\t" \
    "v_mfma_f32_32x32x16_bf16 v[64:79], v[136:139], v[128:131], v[64:79]\n\t" \
    "ds_read_b128 v[144:147], v161 offset:0\n\t" \
    "ds_read_b64_tr_b16 v[152:153], v168 offset:4096\n\t" \
    "ds_read_b64_tr_b16 v[154:155], v169 offset:4096\n\t" \
    "s_waitcnt lgkmcnt(4)\n\t" \
    "v_mfma_f32_32x32x16_bf16 v[80:95], v[140:143], v[128:131], v[80:95]\n\t" \
    "ds_read_b64_tr_b16 v[156:157], v170 offset:4096\n\t" \
    "ds_read_b64_tr_b16 v[158:159], v171 offset:4096\n\t" \
    "ds_read_b128 v[148:151], v161 offset:4096\n\t" \
    "s_waitcnt lgkmcnt(6)\n\t" \
    "v_mfma_f32_32x32x16_bf16 v[96:111], v[136:139], v[132:135], v[96:111]\n\t" \
    "v_mfma_f32_32x32x16_bf16 v[112:127], v[140:143], v[132:135], v[112:127]\n\t" \
    "s_waitcnt lgkmcnt(3)\n\t" \
    "v_mfma_f32_32x32x16_bf16 v[64:79], v[152:155], v[144:147], v[64:79]\n\t" \
    "ds_read_b128 v[128:131], v162 offset:0\n\t" \
    "ds_read_b64_tr_b16 v[136:137], v168 offset:8192\n\t" \
    "ds_read_b64_tr_b16 v[138:139], v169 offset:8192\n\t" \
    "s_waitcnt lgkmcnt(4)\n\t" \
    "v_mfma_f32_32x32x16_bf16 v[80:95], v[156:159], v[144:147], v[80:95]\n\t" \
    "ds_read_b64_tr_b16 v[140:141], v170 offset:8192\n\t" \
    "ds_read_b64_tr_b16 v[142:143], v171 offset:8192\n\t" \
    "ds_read_b128 v[132:135], v162 offset:4096\n\t" \
    "s_waitcnt lgkmcnt(6)\n\t" \
    "v_mfma_f32_32x32x16_bf16 v[96:111], v[152:155], v[148:151], v[96:111]\n\t" \
    "v_mfma_f32_32x32x16_bf16 v[112:127], v[156:159], v[148:151], v[112:127]\n\t" \
    "s_waitcnt lgkmcnt(3)\n\t" \
    "v_mfma_f32_32x32x16_bf16 v[64:79], v[136:139], v[128:131], v[64:79]\n\t" \
    "ds_read_b128 v[144:147], v163 offset:0\n\t" \
    "ds_read_b64_tr_b16 v[152:153], v168 offset:12288\n\t" \
    "ds_read_b64_tr_b16 v[154:155], v169 offset:12288\n\t" \
    "s_waitcnt lgkmcnt(4)\n\t" \
    "v_mfma_f32_32x32x16_bf16 v[80:95], v[140:143], v[128:131], v[80:95]\n\t" \
    "ds_read_b64_tr_b16 v[156:157], v170 offset:12288\n\t" \
    "ds_read_b64_tr_b16 v[158:159], v171 offset:12288\n\t" \
    "ds_read_b128 v[148:151], v163 offset:4096\n\t" \
    "s_waitcnt lgkmcnt(6)\n\t" \
    "v_mfma_f32_32x32x16_bf16 v[96:111], v[136:139], v[132:135], v[96:111]\n\t" \
    "v_mfma_f32_32x32x16_bf16 v[112:127], v[140:143], v[132:135], v[112:127]\n\t" \
    "s_waitcnt lgkmcnt(3)\n\t" \
    "v_mfma_f32_32x32x16_bf16 v[64:79], v[152:155], v[144:147], v[64:79]\n\t" \
    "s_waitcnt lgkmcnt(0)\n\t" \
    "s_barrier\n\t" \
    "ds_read_b128 v[128:131], v160 offset:16384\n\t" \
    "ds_read_b64_tr_b16 v[136:137], v164 offset:0\n\t" \
    "ds_read_b64_tr_b16 v[138:139], v165 offset:0\n\t" \
    "v_mfma_f32_32x32x16_bf16 v[80:95], v[156:159], v[144:147], v[80:95]\n\t" \
    "ds_read_b64_tr_b16 v[140:141], v166 offset:0\n\t" \
    "ds_read_b64_tr_b16 v[142:143], v167 offset:0\n\t" \
    "ds_read_b128 v[132:135], v160 offset:20480\n\t" \
    "v_mfma_f32_32x32x16_bf16 v[96:111], v[152:155], v[148:151], v[96:111]\n\t" \
    "v_mfma_f32_32x32x16_bf16 v[112:127], v[156:159], v[148:151], v[112:127]\n\t" \
    "s_waitcnt lgkmcnt(3)\n\t" \
    "v_mfma_f32_32x32x16_bf16 v[64:79], v[136:139], v[128:131], v[64:79]\n\t" \
    "ds_read_b128 v[144:147], v161 offset:16384\n\t" \
    "ds_read_b64_tr_b16 v[152:153], v164 offset:4096\n\t" \
    "ds_read_b64_tr_b16 v[154:155], v165 offset:4096\n\t" \
    "s_waitcnt lgkmcnt(4)\n\t" \
    "v_mfma_f32_32x32x16_bf16 v[80:95], v[140:143], v[128:131], v[80:95]\n\t" \
    "ds_read_b64_tr_b16 v[156:157], v166 offset:4096\n\t" \
    "ds_read_b64_tr_b16 v[158:159], v167 offset:4096\n\t" \
    "ds_read_b128 v[148:151], v161 offset:20480\n\t" \
    "s_waitcnt lgkmcnt(6)\n\t" \
    "v_mfma_f32_32x32x16_bf16 v[96:111], v[136:139], v[132:135], v[96:111]\n\t" \
    "v_mfma_f32_32x32x16_bf16 v[112:127], v[140:143], v[132:135], v[112:127]\n\t" \
    "s_waitcnt lgkmcnt(3)\n\t" \
    "v_mfma_f32_32x32x16_bf16 v[64:79], v[152:155], v[144:147], v[64:79]\n\t" \
    "ds_read_b128 v[128:131], v162 offset:16384\n\t" \
    "ds_read_b64_tr_b16 v[136:137], v164 offset:8192\n\t" \
    "ds_read_b64_tr_b16 v[138:139], v165 offset:8192\n\t" \
    "s_waitcnt lgkmcnt(4)\n\t" \
    "v_mfma_f32_32x32x16_bf16 v[80:95], v[156:159], v[144:147], v[80:95]\n\t" \
    "ds_read_b64_tr_b16 v[140:141], v166 offset:8192\n\t" \
    "ds_read_b64_tr_b16 v[142:143], v167 offset:8192\n\t" \
    "ds_read_b128 v[132:135], v162 offset:20480\n\t" \
    "s_waitcnt lgkmcnt(6)\n\t" \
    "v_mfma_f32_32x32x16_bf16 v[96:111], v[152:155], v[148:151], v[96:111]\n\t" \
    "v_mfma_f32_32x32x16_bf16 v[112:127], v[156:159], v[148:151], v[112:127]\n\t" \
    "s_waitcnt lgkmcnt(3)\n\t" \
    "v_mfma_f32_32x32x16_bf16 v[64:79], v[136:139], v[128:131], v[64:79]\n\t" \
    "ds_read_b128 v[144:147], v163 offset:16384\n\t" \
    "ds_read_b64_tr_b16 v[152:153], v164 offset:12288\n\t" \
    "ds_read_b64_tr_b16 v[154:155], v165 offset:12288\n\t" \
    "s_waitcnt lgkmcnt(4)\n\t" \
    "v_mfma_f32_32x32x16_bf16 v[80:95], v[140:143], v[128:131], v[80:95]\n\t" \
    "ds_read_b64_tr_b16 v[156:157], v166 offset:12288\n\t" \
    "ds_read_b64_tr_b16 v[158:159], v167 offset:12288\n\t" \
    "ds_read_b128 v[148:151], v163 offset:20480\n\t" \
    "s_waitcnt lgkmcnt(6)\n\t" \
    "v_mfma_f32_32x32x16_bf16 v[96:111], v[136:139], v[132:135], v[96:111]\n\t" \
    "v_mfma_f32_32x32x16_bf16 v[112:127], v[140:143], v[132:135], v[112:127]\n\t" \
    "s_waitcnt lgkmcnt(3)\n\t" \
    "v_mfma_f32_32x32x16_bf16 v[64:79], v[152:155], v[144:147], v[64:79]\n\t" \
    "s_waitcnt lgkmcnt(0)\n\t" \
    "s_barrier\n\t" \
    "ds_read_b128 v[128:131], v160 offset:32768\n\t" \
    "ds_read_b64_tr_b16 v[136:137], v164 offset:16384\n\t" \
    "ds_read_b64_tr_b16 v[138:139], v165 offset:16384\n\t" \
    "v_mfma_f32_32x32x16_bf16 v[80:95], v[156:159], v[144:147], v[80:95]\n\t" \
    "ds_read_b64_tr_b16 v[140:141], v166 offset:16384\n\t" \
    "ds_read_b64_tr_b16 v[142:143], v167 offset:16384\n\t" \
    "ds_read_b128 v[132:135], v160 offset:36864\n\t" \
    "v_mfma_f32_32x32x16_bf16 v[96:111], v[152:155], v[148:151], v[96:111]\n\t" \
    "v_mfma_f32_32x32x16_bf16 v[112:127], v[156:159], v[148:151], v[112:127]\n\t" \
    "s_waitcnt lgkmcnt(3)\n\t" \
    "v_mfma_f32_32x32x16_bf16 v[64:79], v[136:139], v[128:131], v[64:79]\n\t" \
    "ds_read_b128 v[144:147], v161 offset:32768\n\t" \
    "ds_read_b64_tr_b16 v[152:153], v164 offset:20480\n\t" \
    "ds_read_b64_tr_b16 v[154:155], v165 offset:20480\n\t" \
    "s_waitcnt lgkmcnt(4)\n\t" \
    "v_mfma_f32_32x32x16_bf16 v[80:95], v[140:143], v[128:131], v[80:95]\n\t" \
    "ds_read_b64_tr_b16 v[156:157], v166 offset:20480\n\t" \
    "ds_read_b64_tr_b16 v[158:159], v167 offset:20480\n\t" \
    "ds_read_b128 v[148:151], v161 offset:36864\n\t" \
    "s_waitcnt lgkmcnt(6)\n\t" \
    "v_mfma_f32_32x32x16_bf16 v[96:111], v[136:139], v[132:135], v[96:111]\n\t" \
    "v_mfma_f32_32x32x16_bf16 v[112:127], v[140:143], v[132:135], v[112:127]\n\t" \
    "s_waitcnt lgkmcnt(3)\n\t" \
    "v_mfma_f32_32x32x16_bf16 v[64:79], v[152:155], v[144:147], v[64:79]\n\t" \
    "ds_read_b128 v[128:131], v162 offset:32768\n\t" \
    "ds_read_b64_tr_b16 v[136:137], v164 offset:24576\n\t" \
    "ds_read_b64_tr_b16 v[138:139], v165 offset:24576\n\t" \
    "s_waitcnt lgkmcnt(4)\n\t" \
    "v_mfma_f32_32x32x16_bf16 v[80:95], v[156:159], v[144:147], v[80:95]\n\t" \
    "ds_read_b64_tr_b16 v[140:141], v166 offset:24576\n\t" \
    "ds_read_b64_tr_b16 v[142:143], v167 offset:24576\n\t" \
    "ds_read_b128 v[132:135], v162 offset:36864\n\t" \
    "s_waitcnt lgkmcnt(6)\n\t" \
    "v_mfma_f32_32x32x16_bf16 v[96:111], v[152:155], v[148:151], v[96:111]\n\t" \
    "v_mfma_f32_32x32x16_bf16 v[112:127], v[156:159], v[148:151], v[112:127]\n\t" \
    "s_waitcnt lgkmcnt(3)\n\t" \
    "v_mfma_f32_32x32x16_bf16 v[64:79], v[136:139], v[128:131], v[64:79]\n\t" \
    "ds_read_b128 v[144:147], v163 offset:32768\n\t" \
    "ds_read_b64_tr_b16 v[152:153], v164 offset:28672\n\t" \
    "ds_read_b64_tr_b16 v[154:155], v165 offset:28672\n\t" \
    "s_waitcnt lgkmcnt(4)\n\t" \
    "v_mfma_f32_32x32x16_bf16 v[80:95], v[140:143], v[128:131], v[80:95]\n\t" \
    "ds_read_b64_tr_b16 v[156:157], v166 offset:28672\n\t" \
    "ds_read_b64_tr_b16 v[158:159], v167 offset:28672\n\t" \
    "ds_read_b128 v[148:151], v163 offset:36864\n\t" \
    "s_waitcnt lgkmcnt(6)\n\t" \
    "v_mfma_f32_32x32x16_bf16 v[96:111], v[136:139], v[132:135], v[96:111]\n\t" \
    "v_mfma_f32_32x32x16_bf16 v[112:127], v[140:143], v[132:135], v[112:127]\n\t" \
    "s_waitcnt lgkmcnt(3)\n\t" \
    "v_mfma_f32_32x32x16_bf16 v[64:79], v[152:155], v[144:147], v[64:79]\n\t" \
    "s_waitcnt lgkmcnt(0)\n\t" \
    "s_barrier\n\t" \
    "ds_read_b128 v[128:131], v160 offset:49152\n\t" \
    "ds_read_b64_tr_b16 v[136:137], v164 offset:32768\n\t" \
    "ds_read_b64_tr_b16 v[138:139], v165 offset:32768\n\t" \
    "v_mfma_f32_32x32x16_bf16 v[80:95], v[156:159], v[144:147], v[80:95]\n\t" \
    "ds_read_b64_tr_b16 v[140:141], v166 offset:32768\n\t" \
    "ds_read_b64_tr_b16 v[142:143], v167 offset:32768\n\t" \
    "ds_read_b128 v[132:135], v160 offset:53248\n\t" \
    "v_mfma_f32_32x32x16_bf16 v[96:111], v[152:155], v[148:151], v[96:111]\n\t" \
    "v_mfma_f32_32x32x16_bf16 v[112:127], v[156:159], v[148:151], v[112:127]\n\t" \
    "s_waitcnt lgkmcnt(3)\n\t" \
    "v_mfma_f32_32x32x16_bf16 v[64:79], v[136:139], v[128:131], v[64:79]\n\t" \
    "ds_read_b128 v[144:147], v161 offset:49152\n\t" \
    "ds_read_b64_tr_b16 v[152:153], v164 offset:36864\n\t" \
    "ds_read_b64_tr_b16 v[154:155], v165 offset:36864\n\t" \
    "s_waitcnt lgkmcnt(4)\n\t" \
    "v_mfma_f32_32x32x16_bf16 v[80:95], v[140:143], v[128:131], v[80:95]\n\t" \
    "ds_read_b64_tr_b16 v[156:157], v166 offset:36864\n\t" \
    "ds_read_b64_tr_b16 v[158:159], v167 offset:36864\n\t" \
    "ds_read_b128 v[148:151], v161 offset:53248\n\t" \
    "s_waitcnt lgkmcnt(6)\n\t" \
    "v_mfma_f32_32x32x16_bf16 v[96:111], v[136:139], v[132:135], v[96:111]\n\t" \
    "v_mfma_f32_32x32x16_bf16 v[112:127], v[140:143], v[132:135], v[112:127]\n\t" \
    "s_waitcnt lgkmcnt(3)\n\t" \
    "v_mfma_f32_32x32x16_bf16 v[64:79], v[152:155], v[144:147], v[64:79]\n\t" \
    "ds_read_b128 v[128:131], v162 offset:49152\n\t" \
    "ds_read_b64_tr_b16 v[136:137], v164 offset:40960\n\t" \
    "ds_read_b64_tr_b16 v[138:139], v165 offset:40960\n\t" \
    "s_waitcnt lgkmcnt(4)\n\t" \
    "v_mfma_f32_32x32x16_bf16 v[80:95], v[156:159], v[144:147], v[80:95]\n\t" \
    "ds_read_b64_tr_b16 v[140:141], v166 offset:40960\n\t" \
    "ds_read_b64_tr_b16 v[142:143], v167 offset:40960\n\t" \
    "ds_read_b128 v[132:135], v162 offset:53248\n\t" \
    "s_waitcnt lgkmcnt(6)\n\t" \
    "v_mfma_f32_32x32x16_bf16 v[96:111], v[152:155], v[148:151], v[96:111]\n\t" \
    "v_mfma_f32_32x32x16_bf16 v[112:127], v[156:159], v[148:151], v[112:127]\n\t" \
    "s_waitcnt lgkmcnt(3)\n\t" \
    "v_mfma_f32_32x32x16_bf16 v[64:79], v[136:139], v[128:131], v[64:79]\n\t" \
    "ds_read_b128 v[144:147], v163 offset:49152\n\t" \
    "ds_read_b64_tr_b16 v[152:153], v164 offset:45056\n\t" \
    "ds_read_b64_tr_b16 v[154:155], v165 offset:45056\n\t" \
    "s_waitcnt lgkmcnt(4)\n\t" \
    "v_mfma_f32_32x32x16_bf16 v[80:95], v[140:143], v[128:131], v[80:95]\n\t" \
    "ds_read_b64_tr_b16 v[156:157], v166 offset:45056\n\t" \
    "ds_read_b64_tr_b16 v[158:159], v167 offset:45056\n\t" \
    "ds_read_b128 v[148:151], v163 offset:53248\n\t" \
    "s_waitcnt lgkmcnt(6)\n\t" \
    "v_mfma_f32_32x32x16_bf16 v[96:111], v[136:139], v[132:135], v[96:111]\n\t" \
    "v_mfma_f32_32x32x16_bf16 v[112:127], v[140:143], v[132:135], v[112:127]\n\t" \
    "s_waitcnt lgkmcnt(3)\n\t" \
    "v_mfma_f32_32x32x16_bf16 v[64:79], v[152:155], v[144:147], v[64:79]\n\t" \
    "s_waitcnt lgkmcnt(0)\n\t" \
    "s_barrier\n\t" \
    "ds_read_b128 v[128:131], v160 offset:0\n\t" \
    "ds_read_b64_tr_b16 v[136:137], v164 offset:49152\n\t" \
    "ds_read_b64_tr_b16 v[138:139], v165 offset:49152\n\t" \
    "v_mfma_f32_32x32x16_bf16 v[80:95], v[156:159], v[144:147], v[80:95]\n\t" \
    "ds_read_b64_tr_b16 v[140:141], v166 offset:49152\n\t" \
    "ds_read_b64_tr_b16 v[142:143], v167 offset:49152\n\t" \
    "ds_read_b128 v[132:135], v160 offset:4096\n\t" \
    "v_mfma_f32_32x32x16_bf16 v[96:111], v[152:155], v[148:151], v[96:111]\n\t" \
    "v_mfma_f32_32x32x16_bf16 v[112:127], v[156:159], v[148:151], v[112:127]\n\t" \
    "buffer_load_dwordx4 v[172:175], v176, %[rws], 0 offen sc1\n\t" \
    "s_waitcnt lgkmcnt(3)\n\t" \
    "v_mfma_f32_32x32x16_bf16 v[64:79], v[136:139], v[128:131], v[64:79]\n\t" \
    "ds_read_b128 v[144:147], v161 offset:0\n\t" \
    "ds_read_b64_tr_b16 v[152:153], v164 offset:53248\n\t" \
    "ds_read_b64_tr_b16 v[154:155], v165 offset:53248\n\t" \
    "s_waitcnt lgkmcnt(4)\n\t" \
    "v_mfma_f32_32x32x16_bf16 v[80:95], v[140:143], v[128:131], v[80:95]\n\t" \
    "ds_read_b64_tr_b16 v[156:157], v166 offset:53248\n\t" \
    "ds_read_b64_tr_b16 v[158:159], v167 offset:53248\n\t" \
    "ds_read_b128 v[148:151], v161 offset:4096\n\t" \
    "s_waitcnt lgkmcnt(6)\n\t" \
    "v_mfma_f32_32x32x16_bf16 v[96:111], v[136:139], v[132:135], v[96:111]\n\t" \
    "v_mfma_f32_32x32x16_bf16 v[112:127], v[140:143], v[132:135], v[112:127]\n\t" \
    "s_waitcnt lgkmcnt(3)\n\t" \
    "v_mfma_f32_32x32x16_bf16 v[64:79], v[152:155], v[144:147], v[64:79]\n\t" \
    "ds_read_b128 v[128:131], v162 offset:0\n\t" \
    "ds_read_b64_tr_b16 v[136:137], v164 offset:57344\n\t" \
    "ds_read_b64_tr_b16 v[138:139], v165 offset:57344\n\t" \
    "s_waitcnt lgkmcnt(4)\n\t" \
    "v_mfma_f32_32x32x16_bf16 v[80:95], v[156:159], v[144:147], v[80:95]\n\t" \
    "ds_read_b64_tr_b16 v[140:141], v166 offset:57344\n\t" \
    "ds_read_b64_tr_b16 v[142:143], v167 offset:57344\n\t" \
    "ds_read_b128 v[132:135], v162 offset:4096\n\t" \
    "s_waitcnt lgkmcnt(6)\n\t" \
    "v_mfma_f32_32x32x16_bf16 v[96:111], v[152:155], v[148:151], v[96:111]\n\t" \
    "v_mfma_f32_32x32x16_bf16 v[112:127], v[156:159], v[148:151], v[112:127]\n\t" \
    "s_waitcnt lgkmcnt(3)\n\t" \
    "v_mfma_f32_32x32x16_bf16 v[64:79], v[136:139], v[128:131], v[64:79]\n\t" \
    "ds_read_b128 v[144:147], v163 offset:0\n\t" \
    "ds_read_b64_tr_b16 v[152:153], v164 offset:61440\n\t" \
    "ds_read_b64_tr_b16 v[154:155], v165 offset:61440\n\t" \
    "s_waitcnt lgkmcnt(4)\n\t" \
    "v_mfma_f32_32x32x16_bf16 v[80:95], v[140:143], v[128:131], v[80:95]\n\t" \
    "ds_read_b64_tr_b16 v[156:157], v166 offset:61440\n\t" \
    "ds_read_b64_tr_b16 v[158:159], v167 offset:61440\n\t" \
    "ds_read_b128 v[148:151], v163 offset:4096\n\t" \
    "s_waitcnt lgkmcnt(6)\n\t" \
    "v_mfma_f32_32x32x16_bf16 v[96:111], v[136:139], v[132:135], v[96:111]\n\t" \
    "v_mfma_f32_32x32x16_bf16 v[112:127], v[140:143], v[132:135], v[112:127]\n\t" \
    "s_waitcnt lgkmcnt(3)\n\t" \
    "v_mfma_f32_32x32x16_bf16 v[64:79], v[152:155], v[144:147], v[64:79]\n\t" \
    "s_waitcnt lgkmcnt(0)\n\t" \
    "s_barrier\n\t" \
    "ds_read_b128 v[128:131], v160 offset:16384\n\t" \
    "ds_read_b64_tr_b16 v[136:137], v168 offset:0\n\t" \
    "ds_read_b64_tr_b16 v[138:139], v169 offset:0\n\t" \
    "v_mfma_f32_32x32x16_bf16 v[80:95], v[156:159], v[144:147], v[80:95]\n\t" \
    "ds_read_b64_tr_b16 v[140:141], v170 offset:0\n\t" \
    "ds_read_b64_tr_b16 v[142:143], v171 offset:0\n\t" \
    "ds_read_b128 v[132:135], v160 offset:20480\n\t" \
    "v_mfma_f32_32x32x16_bf16 v[96:111], v[152:155], v[148:151], v[96:111]\n\t" \
    "v_mfma_f32_32x32x16_bf16 v[112:127], v[156:159], v[148:151], v[112:127]\n\t" \
    "s_waitcnt lgkmcnt(3)\n\t" \
    "v_mfma_f32_32x32x16_bf16 v[64:79], v[136:139], v[128:131], v[64:79]\n\t" \
    "ds_read_b128 v[144:147], v161 offset:16384\n\t" \
    "ds_read_b64_tr_b16 v[152:153], v168 offset:4096\n\t" \
    "ds_read_b64_tr_b16 v[154:155], v169 offset:4096\n\t" \
    "s_waitcnt lgkmcnt(4)\n\t" \
    "v_mfma_f32_32x32x16_bf16 v[80:95], v[140:143], v[128:131], v[80:95]\n\t" \
    "ds_read_b64_tr_b16 v[156:157], v170 offset:4096\n\t" \
    "ds_read_b64_tr_b16 v[158:159], v171 offset:4096\n\t" \
    "ds_read_b128 v[148:151], v161 offset:20480\n\t" \
    "s_waitcnt lgkmcnt(6)\n\t" \
    "v_mfma_f32_32x32x16_bf16 v[96:111], v[136:139], v[132:135], v[96:111]\n\t" \
    "v_mfma_f32_32x32x16_bf16 v[112:127], v[140:143], v[132:135], v[112:127]\n\t" \
    "s_waitcnt lgkmcnt(3)\n\t" \
    "v_mfma_f32_32x32x16_bf16 v[64:79], v[152:155], v[144:147], v[64:79]\n\t" \
    "ds_read_b128 v[128:131], v162 offset:16384\n\t" \
    "ds_read_b64_tr_b16 v[136:137], v168 offset:8192\n\t" \
    "ds_read_b64_tr_b16 v[138:139], v169 offset:8192\n\t" \
    "s_waitcnt lgkmcnt(4)\n\t" \
    "v_mfma_f32_32x32x16_bf16 v[80:95], v[156:159], v[144:147], v[80:95]\n\t" \
    "ds_read_b64_tr_b16 v[140:141], v170 offset:8192\n\t" \
    "ds_read_b64_tr_b16 v[142:143], v171 offset:8192\n\t" \
    "ds_read_b128 v[132:135], v162 offset:20480\n\t" \
    "s_waitcnt lgkmcnt(6)\n\t" \
    "v_mfma_f32_32x32x16_bf16 v[96:111], v[152:155], v[148:151], v[96:111]\n\t" \
    "v_mfma_f32_32x32x16_bf16 v[112:127], v[156:159], v[148:151], v[112:127]\n\t" \
    "s_waitcnt lgkmcnt(3)\n\t" \
    "v_mfma_f32_32x32x16_bf16 v[64:79], v[136:139], v[128:131], v[64:79]\n\t" \
    "ds_read_b128 v[144:147], v163 offset:16384\n\t" \
    "ds_read_b64_tr_b16 v[152:153], v168 offset:12288\n\t" \
    "ds_read_b64_tr_b16 v[154:155], v169 offset:12288\n\t" \
    "s_waitcnt lgkmcnt(4)\n\t" \
    "v_mfma_f32_32x32x16_bf16 v[80:95], v[140:143], v[128:131], v[80:95]\n\t" \
    "ds_read_b64_tr_b16 v[156:157], v170 offset:12288\n\t" \
    "ds_read_b64_tr_b16 v[158:159], v171 offset:12288\n\t" \
    "ds_read_b128 v[148:151], v163 offset:20480\n\t" \
    "s_waitcnt lgkmcnt(6)\n\t" \
    "v_mfma_f32_32x32x16_bf16 v[96:111], v[136:139], v[132:135], v[96:111]\n\t" \
    "v_mfma_f32_32x32x16_bf16 v[112:127], v[140:143], v[132:135], v[112:127]\n\t" \
    "s_waitcnt lgkmcnt(3)\n\t" \
    "v_mfma_f32_32x32x16_bf16 v[64:79], v[152:155], v[144:147], v[64:79]\n\t" \
    "s_waitcnt lgkmcnt(0)\n\t" \
    "v_mfma_f32_32x32x16_bf16 v[80:95], v[156:159], v[144:147], v[80:95]\n\t" \
    "v_mfma_f32_32x32x16_bf16 v[96:111], v[152:155], v[148:151], v[96:111]\n\t" \
    "v_mfma_f32_32x32x16_bf16 v[112:127], v[156:159], v[148:151], v[112:127]\n\t" \
    "s_waitcnt vmcnt(0)\n\t" \
    "s_nop 15\n\t" \
    ""
#define T1_LOOP_ASM_V \
    "s_barrier\n\t" \
    "ds_read_b128 v[128:131], v160 offset:0\n\t" \
    "ds_read_b64_tr_b16 v[136:137], v164 offset:0\n\t" \
    "ds_read_b64_tr_b16 v[138:139], v165 offset:0\n\t" \
    "ds_read_b64_tr_b16 v[140:141], v166 offset:0\n\t" \
    "ds_read_b64_tr_b16 v[142:143], v167 offset:0\n\t" \
    "ds_read_b128 v[132:135], v160 offset:4096\n\t" \
    "s_waitcnt lgkmcnt(3)\n\t" \
    "v_mfma_f32_32x32x16_bf16 v[64:79], v[128:131], v[136:139], v[64:79]\n\t" \
    "ds_read_b128 v[144:147], v161 offset:0\n\t" \
    "ds_read_b64_tr_b16 v[152:153], v164 offset:4096\n\t" \
    "ds_read_b64_tr_b16 v[154:155], v165 offset:4096\n\t" \
    "s_waitcnt lgkmcnt(4)\n\t" \
    "v_mfma_f32_32x32x16_bf16 v[80:95], v[128:131], v[140:143], v[80:95]\n\t" \
    "ds_read_b64_tr_b16 v[156:157], v166 offset:4096\n\t" \
    "ds_read_b64_tr_b16 v[158:159], v167 offset:4096\n\t" \
    "ds_read_b128 v[148:151], v161 offset:4096\n\t" \
    "s_waitcnt lgkmcnt(6)\n\t" \
    "v_mfma_f32_32x32x16_bf16 v[96:111], v[132:135], v[136:139], v[96:111]\n\t" \
    "v_mfma_f32_32x32x16_bf16 v[112:127], v[132:135], v[140:143], v[112:127]\n\t" \
    "s_waitcnt lgkmcnt(3)\n\t" \
    "v_mfma_f32_32x32x16_bf16 v[64:79], v[144:147], v[152:155], v[64:79]\n\t" \
    "ds_read_b128 v[128:131], v162 offset:0\n\t" \
    "ds_read_b64_tr_b16 v[136:137], v164 offset:8192\n\t" \
    "ds_read_b64_tr_b16 v[138:139], v165 offset:8192\n\t" \
    "s_waitcnt lgkmcnt(4)\n\t" \
    "v_mfma_f32_32x32x16_bf16 v[80:95], v[144:147], v[156:159], v[80:95]\n\t" \
    "ds_read_b64_tr_b16 v[140:141], v166 offset:8192\n\t" \
    "ds_read_b64_tr_b16 v[142:143], v167 offset:8192\n\t" \
    "ds_read_b128 v[132:135], v162 offset:4096\n\t" \
    "s_waitcnt lgkmcnt(6)\n\t" \
    "v_mfma_f32_32x32x16_bf16 v[96:111], v[148:151], v[152:155], v[96:111]\n\t" \
    "v_mfma_f32_32x32x16_bf16 v[112:127], v[148:151], v[156:159], v[112:127]\n\t" \
    "s_waitcnt lgkmcnt(3)\n\t" \
    "v_mfma_f32_32x32x16_bf16 v[64:79], v[128:131], v[136:139], v[64:79]\n\t" \
    "ds_read_b128 v[144:147], v163 offset:0\n\t" \
    "ds_read_b64_tr_b16 v[152:153], v164 offset:12288\n\t" \
    "ds_read_b64_tr_b16 v[154:155], v165 offset:12288\n\t" \
    "s_waitcnt lgkmcnt(4)\n\t" \
    "v_mfma_f32_32x32x16_bf16 v[80:95], v[128:131], v[140:143], v[80:95]\n\t" \
    "ds_read_b64_tr_b16 v[156:157], v166 offset:12288\n\t" \
    "ds_read_b64_tr_b16 v[158:159], v167 offset:12288\n\t" \
    "ds_read_b128 v[148:151], v163 offset:4096\n\t" \
    "s_waitcnt lgkmcnt(6)\n\t" \
    "v_mfma_f32_32x32x16_bf16 v[96:111], v[132:135], v[136:139], v[96:111]\n\t" \
    "v_mfma_f32_32x32x16_bf16 v[112:127], v[132:135], v[140:143], v[112:127]\n\t" \
    "s_waitcnt lgkmcnt(3)\n\t" \
    "v_mfma_f32_32x32x16_bf16 v[64:79], v[144:147], v[152:155], v[64:79]\n\t" \
    "s_waitcnt lgkmcnt(0)\n\t" \
    "s_barrier\n\t" \
    "ds_read_b128 v[128:131], v160 offset:16384\n\t" \
    "ds_read_b64_tr_b16 v[136:137], v164 offset:16384\n\t" \
    "ds_read_b64_tr_b16 v[138:139], v165 offset:16384\n\t" \
    "v_mfma_f32_32x32x16_bf16 v[80:95], v[144:147], v[156:159], v[80:95]\n\t" \
    "ds_read_b64_tr_b16 v[140:141], v166 offset:16384\n\t" \
    "ds_read_b64_tr_b16 v[142:143], v167 offset:16384\n\t" \
    "ds_read_b128 v[132:135], v160 offset:20480\n\t" \
    "v_mfma_f32_32x32x16_bf16 v[96:111], v[148:151], v[152:155], v[96:111]\n\t" \
    "v_mfma_f32_32x32x16_bf16 v[112:127], v[148:151], v[156:159], v[112:127]\n\t" \
    "s_waitcnt lgkmcnt(3)\n\t" \
    "v_mfma_f32_32x32x16_bf16 v[64:79], v[128:131], v[136:139], v[64:79]\n\t" \
    "ds_read_b128 v[144:147], v161 offset:16384\n\t" \
    "ds_read_b64_tr_b16 v[152:153], v164 offset:20480\n\t" \
    "ds_read_b64_tr_b16 v[154:155], v165 offset:20480\n\t" \
    "s_waitcnt lgkmcnt(4)\n\t" \
    "v_mfma_f32_32x32x16_bf16 v[80:95], v[128:131], v[140:143], v[80:95]\n\t" \
    "ds_read_b64_tr_b16 v[156:157], v166 offset:20480\n\t" \
    "ds_read_b64_tr_b16 v[158:159], v167 offset:20480\n\t" \
    "ds_read_b128 v[148:151], v161 offset:20480\n\t" \
    "s_waitcnt lgkmcnt(6)\n\t" \
    "v_mfma_f32_32x32x16_bf16 v[96:111], v[132:135], v[136:139], v[96:111]\n\t" \
    "v_mfma_f32_32x32x16_bf16 v[112:127], v[132:135], v[140:143], v[112:127]\n\t" \
    "s_waitcnt lgkmcnt(3)\n\t" \
    "v_mfma_f32_32x32x16_bf16 v[64:79], v[144:147], v[152:155], v[64:79]\n\t" \
    "ds_read_b128 v[128:131], v162 offset:16384\n\t" \
    "ds_read_b64_tr_b16 v[136:137], v164 offset:24576\n\t" \
    "ds_read_b64_tr_b16 v[138:139], v165 offset:24576\n\t" \
    "s_waitcnt lgkmcnt(4)\n\t" \
    "v_mfma_f32_32x32x16_bf16 v[80:95], v[144:147], v[156:159], v[80:95]\n\t" \
    "ds_read_b64_tr_b16 v[140:141], v166 offset:24576\n\t" \
    "ds_read_b64_tr_b16 v[142:143], v167 offset:24576\n\t" \
    "ds_read_b128 v[132:135], v162 offset:20480\n\t" \
    "s_waitcnt lgkmcnt(6)\n\t" \
    "v_mfma_f32_32x32x16_bf16 v[96:111], v[148:151], v[152:155], v[96:111]\n\t" \
    "v_mfma_f32_32x32x16_bf16 v[112:127], v[148:151], v[156:159], v[112:127]\n\t" \
    "s_waitcnt lgkmcnt(3)\n\t" \
    "v_mfma_f32_32x32x16_bf16 v[64:79], v[128:131], v[136:139], v[64:79]\n\t" \
    "ds_read_b128 v[144:147], v163 offset:16384\n\t" \
    "ds_read_b64_tr_b16 v[152:153], v164 offset:28672\n\t" \
    "ds_read_b64_tr_b16 v[154:155], v165 offset:28672\n\t" \
    "s_waitcnt lgkmcnt(4)\n\t" \
    "v_mfma_f32_32x32x16_bf16 v[80:95], v[128:131], v[140:143], v[80:95]\n\t" \
    "ds_read_b64_tr_b16 v[156:157], v166 offset:28672\n\t" \
    "ds_read_b64_tr_b16 v[158:159], v167 offset:28672\n\t" \
    "ds_read_b128 v[148:151], v163 offset:20480\n\t" \
    "s_waitcnt lgkmcnt(6)\n\t" \
    "v_mfma_f32_32x32x16_bf16 v[96:111], v[132:135], v[136:139], v[96:111]\n\t" \
    "v_mfma_f32_32x32x16_bf16 v[112:127], v[132:135], v[140:143], v[112:127]\n\t" \
    "s_waitcnt lgkmcnt(3)\n\t" \
    "v_mfma_f32_32x32x16_bf16 v[64:79], v[144:147], v[152:155], v[64:79]\n\t" \
    "s_waitcnt lgkmcnt(0)\n\t" \
    "s_barrier\n\t" \
    "ds_read_b128 v[128:131], v160 offset:32768\n\t" \
    "ds_read_b64_tr_b16 v[136:137], v164 offset:32768\n\t" \
    "ds_read_b64_tr_b16 v[138:139], v165 offset:32768\n\t" \
    "v_mfma_f32_32x32x16_bf16 v[80:95], v[144:147], v[156:159], v[80:95]\n\t" \
    "ds_read_b64_tr_b16 v[140:141], v166 offset:32768\n\t" \
    "ds_read_b64_tr_b16 v[142:143], v167 offset:32768\n\t" \
    "ds_read_b128 v[132:135], v160 offset:36864\n\t" \
    "v_mfma_f32_32x32x16_bf16 v[96:111], v[148:151], v[152:155], v[96:111]\n\t" \
    "v_mfma_f32_32x32x16_bf16 v[112:127], v[148:151], v[156:159], v[112:127]\n\t" \
    "s_waitcnt lgkmcnt(3)\n\t" \
    "v_mfma_f32_32x32x16_bf16 v[64:79], v[128:131], v[136:139], v[64:79]\n\t" \
    "ds_read_b128 v[144:147], v161 offset:32768\n\t" \
    "ds_read_b64_tr_b16 v[152:153], v164 offset:36864\n\t" \
    "ds_read_b64_tr_b16 v[154:155], v165 offset:36864\n\t" \
    "s_waitcnt lgkmcnt(4)\n\t" \
    "v_mfma_f32_32x32x16_bf16 v[80:95], v[128:131], v[140:143], v[80:95]\n\t" \
    "ds_read_b64_tr_b16 v[156:157], v166 offset:36864\n\t" \
    "ds_read_b64_tr_b16 v[158:159], v167 offset:36864\n\t" \
    "ds_read_b128 v[148:151], v161 offset:36864\n\t" \
    "s_waitcnt lgkmcnt(6)\n\t" \
    "v_mfma_f32_32x32x16_bf16 v[96:111], v[132:135], v[136:139], v[96:111]\n\t" \
    "v_mfma_f32_32x32x16_bf16 v[112:127], v[132:135], v[140:143], v[112:127]\n\t" \
    "s_waitcnt lgkmcnt(3)\n\t" \
    "v_mfma_f32_32x32x16_bf16 v[64:79], v[144:147], v[152:155], v[64:79]\n\t" \
    "ds_read_b128 v[128:131], v162 offset:32768\n\t" \
    "ds_read_b64_tr_b16 v[136:137], v164 offset:40960\n\t" \
    "ds_read_b64_tr_b16 v[138:139], v165 offset:40960\n\t" \
    "s_waitcnt lgkmcnt(4)\n\t" \
    "v_mfma_f32_32x32x16_bf16 v[80:95], v[144:147], v[156:159], v[80:95]\n\t" \
    "ds_read_b64_tr_b16 v[140:141], v166 offset:40960\n\t" \
    "ds_read_b64_tr_b16 v[142:143], v167 offset:40960\n\t" \
    "ds_read_b128 v[132:135], v162 offset:36864\n\t" \
    "s_waitcnt lgkmcnt(6)\n\t" \
    "v_mfma_f32_32x32x16_bf16 v[96:111], v[148:151], v[152:155], v[96:111]\n\t" \
    "v_mfma_f32_32x32x16_bf16 v[112:127], v[148:151], v[156:159], v[112:127]\n\t" \
    "s_waitcnt lgkmcnt(3)\n\t" \
    "v_mfma_f32_32x32x16_bf16 v[64:79], v[128:131], v[136:139], v[64:79]\n\t" \
    "ds_read_b128 v[144:147], v163 offset:32768\n\t" \
    "ds_read_b64_tr_b16 v[152:153], v164 offset:45056\n\t" \
    "ds_read_b64_tr_b16 v[154:155], v165 offset:45056\n\t" \
    "s_waitcnt lgkmcnt(4)\n\t" \
    "v_mfma_f32_32x32x16_bf16 v[80:95], v[128:131], v[140:143], v[80:95]\n\t" \
    "ds_read_b64_tr_b16 v[156:157], v166 offset:45056\n\t" \
    "ds_read_b64_tr_b16 v[158:159], v167 offset:45056\n\t" \
    "ds_read_b128 v[148:151], v163 offset:36864\n\t" \
    "s_waitcnt lgkmcnt(6)\n\t" \
    "v_mfma_f32_32x32x16_bf16 v[96:111], v[132:135], v[136:139], v[96:111]\n\t" \
    "v_mfma_f32_32x32x16_bf16 v[112:127], v[132:135], v[140:143], v[112:127]\n\t" \
    "s_waitcnt lgkmcnt(3)\n\t" \
    "v_mfma_f32_32x32x16_bf16 v[64:79], v[144:147], v[152:155], v[64:79]\n\t" \
    "s_waitcnt lgkmcnt(0)\n\t" \
    "s_barrier\n\t" \
    "ds_read_b128 v[128:131], v160 offset:49152\n\t" \
    "ds_read_b64_tr_b16 v[136:137], v164 offset:49152\n\t" \
    "ds_read_b64_tr_b16 v[138:139], v165 offset:49152\n\t" \
    "v_mfma_f32_32x32x16_bf16 v[80:95], v[144:147], v[156:159], v[80:95]\n\t" \
    "ds_read_b64_tr_b16 v[140:141], v166 offset:49152\n\t" \
    "ds_read_b64_tr_b16 v[142:143], v167 offset:49152\n\t" \
    "ds_read_b128 v[132:135], v160 offset:53248\n\t" \
    "v_mfma_f32_32x32x16_bf16 v[96:111], v[148:151], v[152:155], v[96:111]\n\t" \
    "v_mfma_f32_32x32x16_bf16 v[112:127], v[148:151], v[156:159], v[112:127]\n\t" \
    "s_waitcnt lgkmcnt(3)\n\t" \
    "v_mfma_f32_32x32x16_bf16 v[64:79], v[128:131], v[136:139], v[64:79]\n\t" \
    "ds_read_b128 v[144:147], v161 offset:49152\n\t" \
    "ds_read_b64_tr_b16 v[152:153], v164 offset:53248\n\t" \
    "ds_read_b64_tr_b16 v[154:155], v165 offset:53248\n\t" \
    "s_waitcnt lgkmcnt(4)\n\t" \
    "v_mfma_f32_32x32x16_bf16 v[80:95], v[128:131], v[140:143], v[80:95]\n\t" \
    "ds_read_b64_tr_b16 v[156:157], v166 offset:53248\n\t" \
    "ds_read_b64_tr_b16 v[158:159], v167 offset:53248\n\t" \
    "ds_read_b128 v[148:151], v161 offset:53248\n\t" \
    "s_waitcnt lgkmcnt(6)\n\t" \
    "v_mfma_f32_32x32x16_bf16 v[96:111], v[132:135], v[136:139], v[96:111]\n\t" \
    "v_mfma_f32_32x32x16_bf16 v[112:127], v[132:135], v[140:143], v[112:127]\n\t" \
    "s_waitcnt lgkmcnt(3)\n\t" \
    "v_mfma_f32_32x32x16_bf16 v[64:79], v[144:147], v[152:155], v[64:79]\n\t" \
    "ds_read_b128 v[128:131], v162 offset:49152\n\t" \
    "ds_read_b64_tr_b16 v[136:137], v164 offset:57344\n\t" \
    "ds_read_b64_tr_b16 v[138:139], v165 offset:57344\n\t" \
    "s_waitcnt lgkmcnt(4)\n\t" \
    "v_mfma_f32_32x32x16_bf16 v[80:95], v[144:147], v[156:159], v[80:95]\n\t" \
    "ds_read_b64_tr_b16 v[140:141], v166 offset:57344\n\t" \
    "ds_read_b64_tr_b16 v[142:143], v167 offset:57344\n\t" \
    "ds_read_b128 v[132:135], v162 offset:53248\n\t" \
    "s_waitcnt lgkmcnt(6)\n\t" \
    "v_mfma_f32_32x32x16_bf16 v[96:111], v[148:151], v[152:155], v[96:111]\n\t" \
    "v_mfma_f32_32x32x16_bf16 v[112:127], v[148:151], v[156:159], v[112:127]\n\t" \
    "s_waitcnt lgkmcnt(3)\n\t" \
    "v_mfma_f32_32x32x16_bf16 v[64:79], v[128:131], v[136:139], v[64:79]\n\t" \
    "ds_read_b128 v[144:147], v163 offset:49152\n\t" \
    "ds_read_b64_tr_b16 v[152:153], v164 offset:61440\n\t" \
    "ds_read_b64_tr_b16 v[154:155], v165 offset:61440\n\t" \
    "s_waitcnt lgkmcnt(4)\n\t" \
    "v_mfma_f32_32x32x16_bf16 v[80:95], v[128:131], v[140:143], v[80:95]\n\t" \
    "ds_read_b64_tr_b16 v[156:157], v166 offset:61440\n\t" \
    "ds_read_b64_tr_b16 v[158:159], v167 offset:61440\n\t" \
    "ds_read_b128 v[148:151], v163 offset:53248\n\t" \
    "s_waitcnt lgkmcnt(6)\n\t" \
    "v_mfma_f32_32x32x16_bf16 v[96:111], v[132:135], v[136:139], v[96:111]\n\t" \
    "v_mfma_f32_32x32x16_bf16 v[112:127], v[132:135], v[140:143], v[112:127]\n\t" \
    "s_waitcnt lgkmcnt(3)\n\t" \
    "v_mfma_f32_32x32x16_bf16 v[64:79], v[144:147], v[152:155], v[64:79]\n\t" \
    "s_waitcnt lgkmcnt(0)\n\t" \
    "s_barrier\n\t" \
    "ds_read_b128 v[128:131], v160 offset:0\n\t" \
    "ds_read_b64_tr_b16 v[136:137], v168 offset:0\n\t" \
    "ds_read_b64_tr_b16 v[138:139], v169 offset:0\n\t" \
    "v_mfma_f32_32x32x16_bf16 v[80:95], v[144:147], v[156:159], v[80:95]\n\t" \
    "ds_read_b64_tr_b16 v[140:141], v170 offset:0\n\t" \
    "ds_read_b64_tr_b16 v[142:143], v171 offset:0\n\t" \
    "ds_read_b128 v[132:135], v160 offset:4096\n\t" \
    "v_mfma_f32_32x32x16_bf16 v[96:111], v[148:151], v[152:155], v[96:111]\n\t" \
    "v_mfma_f32_32x32x16_bf16 v[112:127], v[148:151], v[156:159], v[112:127]\n\t" \
    "s_waitcnt lgkmcnt(3)\n\t" \
    "v_mfma_f32_32x32x16_bf16 v[64:79], v[128:131], v[136:139], v[64:79]\n\t" \
    "ds_read_b128 v[144:147], v161 offset:0\n\t" \
    "ds_read_b64_tr_b16 v[152:153], v168 offset:4096\n\t" \
    "ds_read_b64_tr_b16 v[154:155], v169 offset:4096\n\t" \
    "s_waitcnt lgkmcnt(4)\n\t" \
    "v_mfma_f32_32x32x16_bf16 v[80:95], v[128:131], v[140:143], v[80:95]\n\t" \
    "ds_read_b64_tr_b16 v[156:157], v170 offset:4096\n\t" \
    "ds_read_b64_tr_b16 v[158:159], v171 offset:4096\n\t" \
    "ds_read_b128 v[148:151], v161 offset:4096\n\t" \
    "s_waitcnt lgkmcnt(6)\n\t" \
    "v_mfma_f32_32x32x16_bf16 v[96:111], v[132:135], v[136:139], v[96:111]\n\t" \
    "v_mfma_f32_32x32x16_bf16 v[112:127], v[132:135], v[140:143], v[112:127]\n\t" \
    "s_waitcnt lgkmcnt(3)\n\t" \
    "v_mfma_f32_32x32x16_bf16 v[64:79], v[144:147], v[152:155], v[64:79]\n\t" \
    "ds_read_b128 v[128:131], v162 offset:0\n\t" \
    "ds_read_b64_tr_b16 v[136:137], v168 offset:8192\n\t" \
    "ds_read_b64_tr_b16 v[138:139], v169 offset:8192\n\t" \
    "s_waitcnt lgkmcnt(4)\n\t" \
    "v_mfma_f32_32x32x16_bf16 v[80:95], v[144:147], v[156:159], v[80:95]\n\t" \
    "ds_read_b64_tr_b16 v[140:141], v170 offset:8192\n\t" \
    "ds_read_b64_tr_b16 v[142:143], v171 offset:8192\n\t" \
    "ds_read_b128 v[132:135], v162 offset:4096\n\t" \
    "s_waitcnt lgkmcnt(6)\n\t" \
    "v_mfma_f32_32x32x16_bf16 v[96:111], v[148:151], v[152:155], v[96:111]\n\t" \
    "v_mfma_f32_32x32x16_bf16 v[112:127], v[148:151], v[156:159], v[112:127]\n\t" \
    "s_waitcnt lgkmcnt(3)\n\t" \
    "v_mfma_f32_32x32x16_bf16 v[64:79], v[128:131], v[136:139], v[64:79]\n\t" \
    "ds_read_b128 v[144:147], v163 offset:0\n\t" \
    "ds_read_b64_tr_b16 v[152:153], v168 offset:12288\n\t" \
    "ds_read_b64_tr_b16 v[154:155], v169 offset:12288\n\t" \
    "s_waitcnt lgkmcnt(4)\n\t" \
    "v_mfma_f32_32x32x16_bf16 v[80:95], v[128:131], v[140:143], v[80:95]\n\t" \
    "ds_read_b64_tr_b16 v[156:157], v170 offset:12288\n\t" \
    "ds_read_b64_tr_b16 v[158:159], v171 offset:12288\n\t" \
    "ds_read_b128 v[148:151], v163 offset:4096\n\t" \
    "s_waitcnt lgkmcnt(6)\n\t" \
    "v_mfma_f32_32x32x16_bf16 v[96:111], v[132:135], v[136:139], v[96:111]\n\t" \
    "v_mfma_f32_32x32x16_bf16 v[112:127], v[132:135], v[140:143], v[112:127]\n\t" \
    "s_waitcnt lgkmcnt(3)\n\t" \
    "v_mfma_f32_32x32x16_bf16 v[64:79], v[144:147], v[152:155], v[64:79]\n\t" \
    "s_waitcnt lgkmcnt(0)\n\t" \
    "s_barrier\n\t" \
    "ds_read_b128 v[128:131], v160 offset:16384\n\t" \
    "ds_read_b64_tr_b16 v[136:137], v164 offset:0\n\t" \
    "ds_read_b64_tr_b16 v[138:139], v165 offset:0\n\t" \
    "v_mfma_f32_32x32x16_bf16 v[80:95], v[144:147], v[156:159], v[80:95]\n\t" \
    "ds_read_b64_tr_b16 v[140:141], v166 offset:0\n\t" \
    "ds_read_b64_tr_b16 v[142:143], v167 offset:0\n\t" \
    "ds_read_b128 v[132:135], v160 offset:20480\n\t" \
    "v_mfma_f32_32x32x16_bf16 v[96:111], v[148:151], v[152:155], v[96:111]\n\t" \
    "v_mfma_f32_32x32x16_bf16 v[112:127], v[148:151], v[156:159], v[112:127]\n\t" \
    "s_waitcnt lgkmcnt(3)\n\t" \
    "v_mfma_f32_32x32x16_bf16 v[64:79], v[128:131], v[136:139], v[64:79]\n\t" \
    "ds_read_b128 v[144:147], v161 offset:16384\n\t" \
    "ds_read_b64_tr_b16 v[152:153], v164 offset:4096\n\t" \
    "ds_read_b64_tr_b16 v[154:155], v165 offset:4096\n\t" \
    "s_waitcnt lgkmcnt(4)\n\t" \
    "v_mfma_f32_32x32x16_bf16 v[80:95], v[128:131], v[140:143], v[80:95]\n\t" \
    "ds_read_b64_tr_b16 v[156:157], v166 offset:4096\n\t" \
    "ds_read_b64_tr_b16 v[158:159], v167 offset:4096\n\t" \
    "ds_read_b128 v[148:151], v161 offset:20480\n\t" \
    "s_waitcnt lgkmcnt(6)\n\t" \
    "v_mfma_f32_32x32x16_bf16 v[96:111], v[132:135], v[136:139], v[96:111]\n\t" \
    "v_mfma_f32_32x32x16_bf16 v[112:127], v[132:135], v[140:143], v[112:127]\n\t" \
    "s_waitcnt lgkmcnt(3)\n\t" \
    "v_mfma_f32_32x32x16_bf16 v[64:79], v[144:147], v[152:155], v[64:79]\n\t" \
    "ds_read_b128 v[128:131], v162 offset:16384\n\t" \
    "ds_read_b64_tr_b16 v[136:137], v164 offset:8192\n\t" \
    "ds_read_b64_tr_b16 v[138:139], v165 offset:8192\n\t" \
    "s_waitcnt lgkmcnt(4)\n\t" \
    "v_mfma_f32_32x32x16_bf16 v[80:95], v[144:147], v[156:159], v[80:95]\n\t" \
    "ds_read_b64_tr_b16 v[140:141], v166 offset:8192\n\t" \
    "ds_read_b64_tr_b16 v[142:143], v167 offset:8192\n\t" \
    "ds_read_b128 v[132:135], v162 offset:20480\n\t" \
    "s_waitcnt lgkmcnt(6)\n\t" \
    "v_mfma_f32_32x32x16_bf16 v[96:111], v[148:151], v[152:155], v[96:111]\n\t" \
    "v_mfma_f32_32x32x16_bf16 v[112:127], v[148:151], v[156:159], v[112:127]\n\t" \
    "s_waitcnt lgkmcnt(3)\n\t" \
    "v_mfma_f32_32x32x16_bf16 v[64:79], v[128:131], v[136:139], v[64:79]\n\t" \
    "ds_read_b128 v[144:147], v163 offset:16384\n\t" \
    "ds_read_b64_tr_b16 v[152:153], v164 offset:12288\n\t" \
    "ds_read_b64_tr_b16 v[154:155], v165 offset:12288\n\t" \
    "s_waitcnt lgkmcnt(4)\n\t" \
    "v_mfma_f32_32x32x16_bf16 v[80:95], v[128:131], v[140:143], v[80:95]\n\t" \
    "ds_read_b64_tr_b16 v[156:157], v166 offset:12288\n\t" \
    "ds_read_b64_tr_b16 v[158:159], v167 offset:12288\n\t" \
    "ds_read_b128 v[148:151], v163 offset:20480\n\t" \
    "s_waitcnt lgkmcnt(6)\n\t" \
    "v_mfma_f32_32x32x16_bf16 v[96:111], v[132:135], v[136:139], v[96:111]\n\t" \
    "v_mfma_f32_32x32x16_bf16 v[112:127], v[132:135], v[140:143], v[112:127]\n\t" \
    "s_waitcnt lgkmcnt(3)\n\t" \
    "v_mfma_f32_32x32x16_bf16 v[64:79], v[144:147], v[152:155], v[64:79]\n\t" \
    "s_waitcnt lgkmcnt(0)\n\t" \
    "s_barrier\n\t" \
    "ds_read_b128 v[128:131], v160 offset:32768\n\t" \
    "ds_read_b64_tr_b16 v[136:137], v164 offset:16384\n\t" \
    "ds_read_b64_tr_b16 v[138:139], v165 offset:16384\n\t" \
    "v_mfma_f32_32x32x16_bf16 v[80:95], v[144:147], v[156:159], v[80:95]\n\t" \
    "ds_read_b64_tr_b16 v[140:141], v166 offset:16384\n\t" \
    "ds_read_b64_tr_b16 v[142:143], v167 offset:16384\n\t" \
    "ds_read_b128 v[132:135], v160 offset:36864\n\t" \
    "v_mfma_f32_32x32x16_bf16 v[96:111], v[148:151], v[152:155], v[96:111]\n\t" \
    "v_mfma_f32_32x32x16_bf16 v[112:127], v[148:151], v[156:159], v[112:127]\n\t" \
    "s_waitcnt lgkmcnt(3)\n\t" \
    "v_mfma_f32_32x32x16_bf16 v[64:79], v[128:131], v[136:139], v[64:79]\n\t" \
    "ds_read_b128 v[144:147], v161 offset:32768\n\t" \
    "ds_read_b64_tr_b16 v[152:153], v164 offset:20480\n\t" \
    "ds_read_b64_tr_b16 v[154:155], v165 offset:20480\n\t" \
    "s_waitcnt lgkmcnt(4)\n\t" \
    "v_mfma_f32_32x32x16_bf16 v[80:95], v[128:131], v[140:143], v[80:95]\n\t" \
    "ds_read_b64_tr_b16 v[156:157], v166 offset:20480\n\t" \
    "ds_read_b64_tr_b16 v[158:159], v167 offset:20480\n\t" \
    "ds_read_b128 v[148:151], v161 offset:36864\n\t" \
    "s_waitcnt lgkmcnt(6)\n\t" \
    "v_mfma_f32_32x32x16_bf16 v[96:111], v[132:135], v[136:139], v[96:111]\n\t" \
    "v_mfma_f32_32x32x16_bf16 v[112:127], v[132:135], v[140:143], v[112:127]\n\t" \
    "s_waitcnt lgkmcnt(3)\n\t" \
    "v_mfma_f32_32x32x16_bf16 v[64:79], v[144:147], v[152:155], v[64:79]\n\t" \
    "ds_read_b128 v[128:131], v162 offset:32768\n\t" \
    "ds_read_b64_tr_b16 v[136:137], v164 offset:24576\n\t" \
    "ds_read_b64_tr_b16 v[138:139], v165 offset:24576\n\t" \
    "s_waitcnt lgkmcnt(4)\n\t" \
    "v_mfma_f32_32x32x16_bf16 v[80:95], v[144:147], v[156:159], v[80:95]\n\t" \
    "ds_read_b64_tr_b16 v[140:141], v166 offset:24576\n\t" \
    "ds_read_b64_tr_b16 v[142:143], v167 offset:24576\n\t" \
    "ds_read_b128 v[132:135], v162 offset:36864\n\t" \
    "s_waitcnt lgkmcnt(6)\n\t" \
    "v_mfma_f32_32x32x16_bf16 v[96:111], v[148:151], v[152:155], v[96:111]\n\t" \
    "v_mfma_f32_32x32x16_bf16 v[112:127], v[148:151], v[156:159], v[112:127]\n\t" \
    "s_waitcnt lgkmcnt(3)\n\t" \
    "v_mfma_f32_32x32x16_bf16 v[64:79], v[128:131], v[136:139], v[64:79]\n\t" \
    "ds_read_b128 v[144:147], v163 offset:32768\n\t" \
    "ds_read_b64_tr_b16 v[152:153], v164 offset:28672\n\t" \
    "ds_read_b64_tr_b16 v[154:155], v165 offset:28672\n\t" \
    "s_waitcnt lgkmcnt(4)\n\t" \
    "v_mfma_f32_32x32x16_bf16 v[80:95], v[128:131], v[140:143], v[80:95]\n\t" \
    "ds_read_b64_tr_b16 v[156:157], v166 offset:28672\n\t" \
    "ds_read_b64_tr_b16 v[158:159], v167 offset:28672\n\t" \
    "ds_read_b128 v[148:151], v163 offset:36864\n\t" \
    "s_waitcnt lgkmcnt(6)\n\t" \
    "v_mfma_f32_32x32x16_bf16 v[96:111], v[132:135], v[136:139], v[96:111]\n\t" \
    "v_mfma_f32_32x32x16_bf16 v[112:127], v[132:135], v[140:143], v[112:127]\n\t" \
    "s_waitcnt lgkmcnt(3)\n\t" \
    "v_mfma_f32_32x32x16_bf16 v[64:79], v[144:147], v[152:155], v[64:79]\n\t" \
    "s_waitcnt lgkmcnt(0)\n\t" \
    "s_barrier\n\t" \
    "ds_read_b128 v[128:131], v160 offset:49152\n\t" \
    "ds_read_b64_tr_b16 v[136:137], v164 offset:32768\n\t" \
    "ds_read_b64_tr_b16 v[138:139], v165 offset:32768\n\t" \
    "v_mfma_f32_32x32x16_bf16 v[80:95], v[144:147], v[156:159], v[80:95]\n\t" \
    "ds_read_b64_tr_b16 v[140:141], v166 offset:32768\n\t" \
    "ds_read_b64_tr_b16 v[142:143], v167 offset:32768\n\t" \
    "ds_read_b128 v[132:135], v160 offset:53248\n\t" \
    "v_mfma_f32_32x32x16_bf16 v[96:111], v[148:151], v[152:155], v[96:111]\n\t" \
    "v_mfma_f32_32x32x16_bf16 v[112:127], v[148:151], v[156:159], v[112:127]\n\t" \
    "s_waitcnt lgkmcnt(3)\n\t" \
    "v_mfma_f32_32x32x16_bf16 v[64:79], v[128:131], v[136:139], v[64:79]\n\t" \
    "ds_read_b128 v[144:147], v161 offset:49152\n\t" \
    "ds_read_b64_tr_b16 v[152:153], v164 offset:36864\n\t" \
    "ds_read_b64_tr_b16 v[154:155], v165 offset:36864\n\t" \
    "s_waitcnt lgkmcnt(4)\n\t" \
    "v_mfma_f32_32x32x16_bf16 v[80:95], v[128:131], v[140:143], v[80:95]\n\t" \
    "ds_read_b64_tr_b16 v[156:157], v166 offset:36864\n\t" \
    "ds_read_b64_tr_b16 v[158:159], v167 offset:36864\n\t" \
    "ds_read_b128 v[148:151], v161 offset:53248\n\t" \
    "s_waitcnt lgkmcnt(6)\n\t" \
    "v_mfma_f32_32x32x16_bf16 v[96:111], v[132:135], v[136:139], v[96:111]\n\t" \
    "v_mfma_f32_32x32x16_bf16 v[112:127], v[132:135], v[140:143], v[112:127]\n\t" \
    "s_waitcnt lgkmcnt(3)\n\t" \
    "v_mfma_f32_32x32x16_bf16 v[64:79], v[144:147], v[152:155], v[64:79]\n\t" \
    "ds_read_b128 v[128:131], v162 offset:49152\n\t" \
    "ds_read_b64_tr_b16 v[136:137], v164 offset:40960\n\t" \
    "ds_read_b64_tr_b16 v[138:139], v165 offset:40960\n\t" \
    "s_waitcnt lgkmcnt(4)\n\t" \
    "v_mfma_f32_32x32x16_bf16 v[80:95], v[144:147], v[156:159], v[80:95]\n\t" \
    "ds_read_b64_tr_b16 v[140:141], v166 offset:40960\n\t" \
    "ds_read_b64_tr_b16 v[142:143], v167 offset:40960\n\t" \
    "ds_read_b128 v[132:135], v162 offset:53248\n\t" \
    "s_waitcnt lgkmcnt(6)\n\t" \
    "v_mfma_f32_32x32x16_bf16 v[96:111], v[148:151], v[152:155], v[96:111]\n\t" \
    "v_mfma_f32_32x32x16_bf16 v[112:127], v[148:151], v[156:159], v[112:127]\n\t" \
    "s_waitcnt lgkmcnt(3)\n\t" \
    "v_mfma_f32_32x32x16_bf16 v[64:79], v[128:131], v[136:139], v[64:79]\n\t" \
    "ds_read_b128 v[144:147], v163 offset:49152\n\t" \
    "ds_read_b64_tr_b16 v[152:153], v164 offset:45056\n\t" \
    "ds_read_b64_tr_b16 v[154:155], v165 offset:45056\n\t" \
    "s_waitcnt lgkmcnt(4)\n\t" \
    "v_mfma_f32_32x32x16_bf16 v[80:95], v[128:131], v[140:143], v[80:95]\n\t" \
    "ds_read_b64_tr_b16 v[156:157], v166 offset:45056\n\t" \
    "ds_read_b64_tr_b16 v[158:159], v167 offset:45056\n\t" \
    "ds_read_b128 v[148:151], v163 offset:53248\n\t" \
    "s_waitcnt lgkmcnt(6)\n\t" \
    "v_mfma_f32_32x32x16_bf16 v[96:111], v[132:135], v[136:139], v[96:111]\n\t" \
    "v_mfma_f32_32x32x16_bf16 v[112:127], v[132:135], v[140:143], v[112:127]\n\t" \
    "s_waitcnt lgkmcnt(3)\n\t" \
    "v_mfma_f32_32x32x16_bf16 v[64:79], v[144:147], v[152:155], v[64:79]\n\t" \
    "s_waitcnt lgkmcnt(0)\n\t" \
    "s_barrier\n\t" \
    "ds_read_b128 v[128:131], v160 offset:0\n\t" \
    "ds_read_b64_tr_b16 v[136:137], v164 offset:49152\n\t" \
    "ds_read_b64_tr_b16 v[138:139], v165 offset:49152\n\t" \
    "v_mfma_f32_32x32x16_bf16 v[80:95], v[144:147], v[156:159], v[80:95]\n\t" \
    "ds_read_b64_tr_b16 v[140:141], v166 offset:49152\n\t" \
    "ds_read_b64_tr_b16 v[142:143], v167 offset:49152\n\t" \
    "ds_read_b128 v[132:135], v160 offset:4096\n\t" \
    "v_mfma_f32_32x32x16_bf16 v[96:111], v[148:151], v[152:155], v[96:111]\n\t" \
    "v_mfma_f32_32x32x16_bf16 v[112:127], v[148:151], v[156:159], v[112:127]\n\t" \
    "buffer_load_dwordx4 v[172:175], v176, %[rws], 0 offen sc1\n\t" \
    "s_waitcnt lgkmcnt(3)\n\t" \
    "v_mfma_f32_32x32x16_bf16 v[64:79], v[128:131], v[136:139], v[64:79]\n\t" \
    "ds_read_b128 v[144:147], v161 offset:0\n\t" \
    "ds_read_b64_tr_b16 v[152:153], v164 offset:53248\n\t" \
    "ds_read_b64_tr_b16 v[154:155], v165 offset:53248\n\t" \
    "s_waitcnt lgkmcnt(4)\n\t" \
    "v_mfma_f32_32x32x16_bf16 v[80:95], v[128:131], v[140:143], v[80:95]\n\t" \
    "ds_read_b64_tr_b16 v[156:157], v166 offset:53248\n\t" \
    "ds_read_b64_tr_b16 v[158:159], v167 offset:53248\n\t" \
    "ds_read_b128 v[148:151], v161 offset:4096\n\t" \
    "s_waitcnt lgkmcnt(6)\n\t" \
    "v_mfma_f32_32x32x16_bf16 v[96:111], v[132:135], v[136:139], v[96:111]\n\t" \
    "v_mfma_f32_32x32x16_bf16 v[112:127], v[132:135], v[140:143], v[112:127]\n\t" \
    "s_waitcnt lgkmcnt(3)\n\t" \
    "v_mfma_f32_32x32x16_bf16 v[64:79], v[144:147], v[152:155], v[64:79]\n\t" \
    "ds_read_b128 v[128:131], v162 offset:0\n\t" \
    "ds_read_b64_tr_b16 v[136:137], v164 offset:57344\n\t" \
    "ds_read_b64_tr_b16 v[138:139], v165 offset:57344\n\t" \
    "s_waitcnt lgkmcnt(4)\n\t" \
    "v_mfma_f32_32x32x16_bf16 v[80:95], v[144:147], v[156:159], v[80:95]\n\t" \
    "ds_read_b64_tr_b16 v[140:141], v166 offset:57344\n\t" \
    "ds_read_b64_tr_b16 v[142:143], v167 offset:57344\n\t" \
    "ds_read_b128 v[132:135], v162 offset:4096\n\t" \
    "s_waitcnt lgkmcnt(6)\n\t" \
    "v_mfma_f32_32x32x16_bf16 v[96:111], v[148:151], v[152:155], v[96:111]\n\t" \
    "v_mfma_f32_32x32x16_bf16 v[112:127], v[148:151], v[156:159], v[112:127]\n\t" \
    "s_waitcnt lgkmcnt(3)\n\t" \
    "v_mfma_f32_32x32x16_bf16 v[64:79], v[128:131], v[136:139], v[64:79]\n\t" \
    "ds_read_b128 v[144:147], v163 offset:0\n\t" \
    "ds_read_b64_tr_b16 v[152:153], v164 offset:61440\n\t" \
    "ds_read_b64_tr_b16 v[154:155], v165 offset:61440\n\t" \
    "s_waitcnt lgkmcnt(4)\n\t" \
    "v_mfma_f32_32x32x16_bf16 v[80:95], v[128:131], v[140:143], v[80:95]\n\t" \
    "ds_read_b64_tr_b16 v[156:157], v166 offset:61440\n\t" \
    "ds_read_b64_tr_b16 v[158:159], v167 offset:61440\n\t" \
    "ds_read_b128 v[148:151], v163 offset:4096\n\t" \
    "s_waitcnt lgkmcnt(6)\n\t" \
    "v_mfma_f32_32x32x16_bf16 v[96:111], v[132:135], v[136:139], v[96:111]\n\t" \
    "v_mfma_f32_32x32x16_bf16 v[112:127], v[132:135], v[140:143], v[112:127]\n\t" \
    "s_waitcnt lgkmcnt(3)\n\t" \
    "v_mfma_f32_32x32x16_bf16 v[64:79], v[144:147], v[152:155], v[64:79]\n\t" \
    "s_waitcnt lgkmcnt(0)\n\t" \
    "s_barrier\n\t" \
    "ds_read_b128 v[128:131], v160 offset:16384\n\t" \
    "ds_read_b64_tr_b16 v[136:137], v168 offset:0\n\t" \
    "ds_read_b64_tr_b16 v[138:139], v169 offset:0\n\t" \
    "v_mfma_f32_32x32x16_bf16 v[80:95], v[144:147], v[156:159], v[80:95]\n\t" \
    "ds_read_b64_tr_b16 v[140:141], v170 offset:0\n\t" \
    "ds_read_b64_tr_b16 v[142:143], v171 offset:0\n\t" \
    "ds_read_b128 v[132:135], v160 offset:20480\n\t" \
    "v_mfma_f32_32x32x16_bf16 v[96:111], v[148:151], v[152:155], v[96:111]\n\t" \
    "v_mfma_f32_32x32x16_bf16 v[112:127], v[148:151], v[156:159], v[112:127]\n\t" \
    "s_waitcnt lgkmcnt(3)\n\t" \
    "v_mfma_f32_32x32x16_bf16 v[64:79], v[128:131], v[136:139], v[64:79]\n\t" \
    "ds_read_b128 v[144:147], v161 offset:16384\n\t" \
    "ds_read_b64_tr_b16 v[152:153], v168 offset:4096\n\t" \
    "ds_read_b64_tr_b16 v[154:155], v169 offset:4096\n\t" \
    "s_waitcnt lgkmcnt(4)\n\t" \
    "v_mfma_f32_32x32x16_bf16 v[80:95], v[128:131], v[140:143], v[80:95]\n\t" \
    "ds_read_b64_tr_b16 v[156:157], v170 offset:4096\n\t" \
    "ds_read_b64_tr_b16 v[158:159], v171 offset:4096\n\t" \
    "ds_read_b128 v[148:151], v161 offset:20480\n\t" \
    "s_waitcnt lgkmcnt(6)\n\t" \
    "v_mfma_f32_32x32x16_bf16 v[96:111], v[132:135], v[136:139], v[96:111]\n\t" \
    "v_mfma_f32_32x32x16_bf16 v[112:127], v[132:135], v[140:143], v[112:127]\n\t" \
    "s_waitcnt lgkmcnt(3)\n\t" \
    "v_mfma_f32_32x32x16_bf16 v[64:79], v[144:147], v[152:155], v[64:79]\n\t" \
    "ds_read_b128 v[128:131], v162 offset:16384\n\t" \
    "ds_read_b64_tr_b16 v[136:137], v168 offset:8192\n\t" \
    "ds_read_b64_tr_b16 v[138:139], v169 offset:8192\n\t" \
    "s_waitcnt lgkmcnt(4)\n\t" \
    "v_mfma_f32_32x32x16_bf16 v[80:95], v[144:147], v[156:159], v[80:95]\n\t" \
    "ds_read_b64_tr_b16 v[140:141], v170 offset:8192\n\t" \
    "ds_read_b64_tr_b16 v[142:143], v171 offset:8192\n\t" \
    "ds_read_b128 v[132:135], v162 offset:20480\n\t" \
    "s_waitcnt lgkmcnt(6)\n\t" \
    "v_mfma_f32_32x32x16_bf16 v[96:111], v[148:151], v[152:155], v[96:111]\n\t" \
    "v_mfma_f32_32x32x16_bf16 v[112:127], v[148:151], v[156:159], v[112:127]\n\t" \
    "s_waitcnt lgkmcnt(3)\n\t" \
    "v_mfma_f32_32x32x16_bf16 v[64:79], v[128:131], v[136:139], v[64:79]\n\t" \
    "ds_read_b128 v[144:147], v163 offset:16384\n\t" \
    "ds_read_b64_tr_b16 v[152:153], v168 offset:12288\n\t" \
    "ds_read_b64_tr_b16 v[154:155], v169 offset:12288\n\t" \
    "s_waitcnt lgkmcnt(4)\n\t" \
    "v_mfma_f32_32x32x16_bf16 v[80:95], v[128:131], v[140:143], v[80:95]\n\t" \
    "ds_read_b64_tr_b16 v[156:157], v170 offset:12288\n\t" \
    "ds_read_b64_tr_b16 v[158:159], v171 offset:12288\n\t" \
    "ds_read_b128 v[148:151], v163 offset:20480\n\t" \
    "s_waitcnt lgkmcnt(6)\n\t" \
    "v_mfma_f32_32x32x16_bf16 v[96:111], v[132:135], v[136:139], v[96:111]\n\t" \
    "v_mfma_f32_32x32x16_bf16 v[112:127], v[132:135], v[140:143], v[112:127]\n\t" \
    "s_waitcnt lgkmcnt(3)\n\t" \
    "v_mfma_f32_32x32x16_bf16 v[64:79], v[144:147], v[152:155], v[64:79]\n\t" \
    "s_waitcnt lgkmcnt(0)\n\t" \
    "v_mfma_f32_32x32x16_bf16 v[80:95], v[144:147], v[156:159], v[80:95]\n\t" \
    "v_mfma_f32_32x32x16_bf16 v[96:111], v[148:151], v[152:155], v[96:111]\n\t" \
    "v_mfma_f32_32x32x16_bf16 v[112:127], v[148:151], v[156:159], v[112:127]\n\t" \
    "s_waitcnt vmcnt(0)\n\t" \
    "s_nop 15\n\t" \
    ""
#define T1_LOOP_ASM_F \
    "s_barrier\n\t" \
    "ds_read_b128 v[128:131], v160 offset:0\n\t" \
    "ds_read_b64_tr_b16 v[136:137], v164 offset:0\n\t" \
    "ds_read_b64_tr_b16 v[138:139], v165 offset:0\n\t" \
    "ds_read_b64_tr_b16 v[140:141], v166 offset:0\n\t" \
    "ds_read_b64_tr_b16 v[142:143], v167 offset:0\n\t" \
    "ds_read_b128 v[132:135], v160 offset:512\n\t" \
    "s_waitcnt lgkmcnt(3)\n\t" \
    "v_mfma_f32_32x32x16_bf16 v[64:79], v[136:139], v[128:131], v[64:79]\n\t" \
    "ds_read_b128 v[144:147], v160 offset:4096\n\t" \
    "ds_read_b64_tr_b16 v[152:153], v164 offset:4096\n\t" \
    "ds_read_b64_tr_b16 v[154:155], v165 offset:4096\n\t" \
    "s_waitcnt lgkmcnt(4)\n\t" \
    "v_mfma_f32_32x32x16_bf16 v[80:95], v[140:143], v[128:131], v[80:95]\n\t" \
    "ds_read_b64_tr_b16 v[156:157], v166 offset:4096\n\t" \
    "ds_read_b64_tr_b16 v[158:159], v167 offset:4096\n\t" \
    "ds_read_b128 v[148:151], v160 offset:4608\n\t" \
    "s_waitcnt lgkmcnt(6)\n\t" \
    "v_mfma_f32_32x32x16_bf16 v[96:111], v[136:139], v[132:135], v[96:111]\n\t" \
    "v_mfma_f32_32x32x16_bf16 v[112:127], v[140:143], v[132:135], v[112:127]\n\t" \
    "s_waitcnt lgkmcnt(3)\n\t" \
    "v_mfma_f32_32x32x16_bf16 v[64:79], v[152:155], v[144:147], v[64:79]\n\t" \
    "ds_read_b128 v[128:131], v160 offset:8192\n\t" \
    "ds_read_b64_tr_b16 v[136:137], v164 offset:8192\n\t" \
    "ds_read_b64_tr_b16 v[138:139], v165 offset:8192\n\t" \
    "s_waitcnt lgkmcnt(4)\n\t" \
    "v_mfma_f32_32x32x16_bf16 v[80:95], v[156:159], v[144:147], v[80:95]\n\t" \
    "ds_read_b64_tr_b16 v[140:141], v166 offset:8192\n\t" \
    "ds_read_b64_tr_b16 v[142:143], v167 offset:8192\n\t" \
    "ds_read_b128 v[132:135], v160 offset:8704\n\t" \
    "s_waitcnt lgkmcnt(6)\n\t" \
    "v_mfma_f32_32x32x16_bf16 v[96:111], v[152:155], v[148:151], v[96:111]\n\t" \
    "v_mfma_f32_32x32x16_bf16 v[112:127], v[156:159], v[148:151], v[112:127]\n\t" \
    "s_waitcnt lgkmcnt(3)\n\t" \
    "v_mfma_f32_32x32x16_bf16 v[64:79], v[136:139], v[128:131], v[64:79]\n\t" \
    "ds_read_b128 v[144:147], v160 offset:12288\n\t" \
    "ds_read_b64_tr_b16 v[152:153], v164 offset:12288\n\t" \
    "ds_read_b64_tr_b16 v[154:155], v165 offset:12288\n\t" \
    "s_waitcnt lgkmcnt(4)\n\t" \
    "v_mfma_f32_32x32x16_bf16 v[80:95], v[140:143], v[128:131], v[80:95]\n\t" \
    "ds_read_b64_tr_b16 v[156:157], v166 offset:12288\n\t" \
    "ds_read_b64_tr_b16 v[158:159], v167 offset:12288\n\t" \
    "ds_read_b128 v[148:151], v160 offset:12800\n\t" \
    "s_waitcnt lgkmcnt(6)\n\t" \
    "v_mfma_f32_32x32x16_bf16 v[96:111], v[136:139], v[132:135], v[96:111]\n\t" \
    "v_mfma_f32_32x32x16_bf16 v[112:127], v[140:143], v[132:135], v[112:127]\n\t" \
    "s_waitcnt lgkmcnt(3)\n\t" \
    "v_mfma_f32_32x32x16_bf16 v[64:79], v[152:155], v[144:147], v[64:79]\n\t" \
    "s_waitcnt lgkmcnt(0)\n\t" \
    "s_barrier\n\t" \
    "ds_read_b128 v[128:131], v160 offset:16384\n\t" \
    "ds_read_b64_tr_b16 v[136:137], v164 offset:16384\n\t" \
    "ds_read_b64_tr_b16 v[138:139], v165 offset:16384\n\t" \
    "v_mfma_f32_32x32x16_bf16 v[80:95], v[156:159], v[144:147], v[80:95]\n\t" \
    "ds_read_b64_tr_b16 v[140:141], v166 offset:16384\n\t" \
    "ds_read_b64_tr_b16 v[142:143], v167 offset:16384\n\t" \
    "ds_read_b128 v[132:135], v160 offset:16896\n\t" \
    "v_mfma_f32_32x32x16_bf16 v[96:111], v[152:155], v[148:151], v[96:111]\n\t" \
    "v_mfma_f32_32x32x16_bf16 v[112:127], v[156:159], v[148:151], v[112:127]\n\t" \
    "s_waitcnt lgkmcnt(3)\n\t" \
    "v_mfma_f32_32x32x16_bf16 v[64:79], v[136:139], v[128:131], v[64:79]\n\t" \
    "ds_read_b128 v[144:147], v160 offset:20480\n\t" \
    "ds_read_b64_tr_b16 v[152:153], v164 offset:20480\n\t" \
    "ds_read_b64_tr_b16 v[154:155], v165 offset:20480\n\t" \
    "s_waitcnt lgkmcnt(4)\n\t" \
    "v_mfma_f32_32x32x16_bf16 v[80:95], v[140:143], v[128:131], v[80:95]\n\t" \
    "ds_read_b64_tr_b16 v[156:157], v166 offset:20480\n\t" \
    "ds_read_b64_tr_b16 v[158:159], v167 offset:20480\n\t" \
    "ds_read_b128 v[148:151], v160 offset:20992\n\t" \
    "s_waitcnt lgkmcnt(6)\n\t" \
    "v_mfma_f32_32x32x16_bf16 v[96:111], v[136:139], v[132:135], v[96:111]\n\t" \
    "v_mfma_f32_32x32x16_bf16 v[112:127], v[140:143], v[132:135], v[112:127]\n\t" \
    "s_waitcnt lgkmcnt(3)\n\t" \
    "v_mfma_f32_32x32x16_bf16 v[64:79], v[152:155], v[144:147], v[64:79]\n\t" \
    "ds_read_b128 v[128:131], v160 offset:24576\n\t" \
    "ds_read_b64_tr_b16 v[136:137], v164 offset:24576\n\t" \
    "ds_read_b64_tr_b16 v[138:139], v165 offset:24576\n\t" \
    "s_waitcnt lgkmcnt(4)\n\t" \
    "v_mfma_f32_32x32x16_bf16 v[80:95], v[156:159], v[144:147], v[80:95]\n\t" \
    "ds_read_b64_tr_b16 v[140:141], v166 offset:24576\n\t" \
    "ds_read_b64_tr_b16 v[142:143], v167 offset:24576\n\t" \
    "ds_read_b128 v[132:135], v160 offset:25088\n\t" \
    "s_waitcnt lgkmcnt(6)\n\t" \
    "v_mfma_f32_32x32x16_bf16 v[96:111], v[152:155], v[148:151], v[96:111]\n\t" \
    "v_mfma_f32_32x32x16_bf16 v[112:127], v[156:159], v[148:151], v[112:127]\n\t" \
    "s_waitcnt lgkmcnt(3)\n\t" \
    "v_mfma_f32_32x32x16_bf16 v[64:79], v[136:139], v[128:131], v[64:79]\n\t" \
    "ds_read_b128 v[144:147], v160 offset:28672\n\t" \
    "ds_read_b64_tr_b16 v[152:153], v164 offset:28672\n\t" \
    "ds_read_b64_tr_b16 v[154:155], v165 offset:28672\n\t" \
    "s_waitcnt lgkmcnt(4)\n\t" \
    "v_mfma_f32_32x32x16_bf16 v[80:95], v[140:143], v[128:131], v[80:95]\n\t" \
    "ds_read_b64_tr_b16 v[156:157], v166 offset:28672\n\t" \
    "ds_read_b64_tr_b16 v[158:159], v167 offset:28672\n\t" \
    "ds_read_b128 v[148:151], v160 offset:29184\n\t" \
    "s_waitcnt lgkmcnt(6)\n\t" \
    "v_mfma_f32_32x32x16_bf16 v[96:111], v[136:139], v[132:135], v[96:111]\n\t" \
    "v_mfma_f32_32x32x16_bf16 v[112:127], v[140:143], v[132:135], v[112:127]\n\t" \
    "s_waitcnt lgkmcnt(3)\n\t" \
    "v_mfma_f32_32x32x16_bf16 v[64:79], v[152:155], v[144:147], v[64:79]\n\t" \
    "s_waitcnt lgkmcnt(0)\n\t" \
    "s_barrier\n\t" \
    "ds_read_b128 v[128:131], v160 offset:32768\n\t" \
    "ds_read_b64_tr_b16 v[136:137], v164 offset:32768\n\t" \
    "ds_read_b64_tr_b16 v[138:139], v165 offset:32768\n\t" \
    "v_mfma_f32_32x32x16_bf16 v[80:95], v[156:159], v[144:147], v[80:95]\n\t" \
    "ds_read_b64_tr_b16 v[140:141], v166 offset:32768\n\t" \
    "ds_read_b64_tr_b16 v[142:143], v167 offset:32768\n\t" \
    "ds_read_b128 v[132:135], v160 offset:33280\n\t" \
    "v_mfma_f32_32x32x16_bf16 v[96:111], v[152:155], v[148:151], v[96:111]\n\t" \
    "v_mfma_f32_32x32x16_bf16 v[112:127], v[156:159], v[148:151], v[112:127]\n\t" \
    "s_waitcnt lgkmcnt(3)\n\t" \
    "v_mfma_f32_32x32x16_bf16 v[64:79], v[136:139], v[128:131], v[64:79]\n\t" \
    "ds_read_b128 v[144:147], v160 offset:36864\n\t" \
    "ds_read_b64_tr_b16 v[152:153], v164 offset:36864\n\t" \
    "ds_read_b64_tr_b16 v[154:155], v165 offset:36864\n\t" \
    "s_waitcnt lgkmcnt(4)\n\t" \
    "v_mfma_f32_32x32x16_bf16 v[80:95], v[140:143], v[128:131], v[80:95]\n\t" \
    "ds_read_b64_tr_b16 v[156:157], v166 offset:36864\n\t" \
    "ds_read_b64_tr_b16 v[158:159], v167 offset:36864\n\t" \
    "ds_read_b128 v[148:151], v160 offset:37376\n\t" \
    "s_waitcnt lgkmcnt(6)\n\t" \
    "v_mfma_f32_32x32x16_bf16 v[96:111], v[136:139], v[132:135], v[96:111]\n\t" \
    "v_mfma_f32_32x32x16_bf16 v[112:127], v[140:143], v[132:135], v[112:127]\n\t" \
    "s_waitcnt lgkmcnt(3)\n\t" \
    "v_mfma_f32_32x32x16_bf16 v[64:79], v[152:155], v[144:147], v[64:79]\n\t" \
    "ds_read_b128 v[128:131], v160 offset:40960\n\t" \
    "ds_read_b64_tr_b16 v[136:137], v164 offset:40960\n\t" \
    "ds_read_b64_tr_b16 v[138:139], v165 offset:40960\n\t" \
    "s_waitcnt lgkmcnt(4)\n\t" \
    "v_mfma_f32_32x32x16_bf16 v[80:95], v[156:159], v[144:147], v[80:95]\n\t" \
    "ds_read_b64_tr_b16 v[140:141], v166 offset:40960\n\t" \
    "ds_read_b64_tr_b16 v[142:143], v167 offset:40960\n\t" \
    "ds_read_b128 v[132:135], v160 offset:41472\n\t" \
    "s_waitcnt lgkmcnt(6)\n\t" \
    "v_mfma_f32_32x32x16_bf16 v[96:111], v[152:155], v[148:151], v[96:111]\n\t" \
    "v_mfma_f32_32x32x16_bf16 v[112:127], v[156:159], v[148:151], v[112:127]\n\t" \
    "s_waitcnt lgkmcnt(3)\n\t" \
    "v_mfma_f32_32x32x16_bf16 v[64:79], v[136:139], v[128:131], v[64:79]\n\t" \
    "ds_read_b128 v[144:147], v160 offset:45056\n\t" \
    "ds_read_b64_tr_b16 v[152:153], v164 offset:45056\n\t" \
    "ds_read_b64_tr_b16 v[154:155], v165 offset:45056\n\t" \
    "s_waitcnt lgkmcnt(4)\n\t" \
    "v_mfma_f32_32x32x16_bf16 v[80:95], v[140:143], v[128:131], v[80:95]\n\t" \
    "ds_read_b64_tr_b16 v[156:157], v166 offset:45056\n\t" \
    "ds_read_b64_tr_b16 v[158:159], v167 offset:45056\n\t" \
    "ds_read_b128 v[148:151], v160 offset:45568\n\t" \
    "s_waitcnt lgkmcnt(6)\n\t" \
    "v_mfma_f32_32x32x16_bf16 v[96:111], v[136:139], v[132:135], v[96:111]\n\t" \
    "v_mfma_f32_32x32x16_bf16 v[112:127], v[140:143], v[132:135], v[112:127]\n\t" \
    "s_waitcnt lgkmcnt(3)\n\t" \
    "v_mfma_f32_32x32x16_bf16 v[64:79], v[152:155], v[144:147], v[64:79]\n\t" \
    "s_waitcnt lgkmcnt(0)\n\t" \
    "s_barrier\n\t" \
    "ds_read_b128 v[128:131], v160 offset:49152\n\t" \
    "ds_read_b64_tr_b16 v[136:137], v164 offset:49152\n\t" \
    "ds_read_b64_tr_b16 v[138:139], v165 offset:49152\n\t" \
    "v_mfma_f32_32x32x16_bf16 v[80:95], v[156:159], v[144:147], v[80:95]\n\t" \
    "ds_read_b64_tr_b16 v[140:141], v166 offset:49152\n\t" \
    "ds_read_b64_tr_b16 v[142:143], v167 offset:49152\n\t" \
    "ds_read_b128 v[132:135], v160 offset:49664\n\t" \
    "v_mfma_f32_32x32x16_bf16 v[96:111], v[152:155], v[148:151], v[96:111]\n\t" \
    "v_mfma_f32_32x32x16_bf16 v[112:127], v[156:159], v[148:151], v[112:127]\n\t" \
    "s_waitcnt lgkmcnt(3)\n\t" \
    "v_mfma_f32_32x32x16_bf16 v[64:79], v[136:139], v[128:131], v[64:79]\n\t" \
    "ds_read_b128 v[144:147], v160 offset:53248\n\t" \
    "ds_read_b64_tr_b16 v[152:153], v164 offset:53248\n\t" \
    "ds_read_b64_tr_b16 v[154:155], v165 offset:53248\n\t" \
    "s_waitcnt lgkmcnt(4)\n\t" \
    "v_mfma_f32_32x32x16_bf16 v[80:95], v[140:143], v[128:131], v[80:95]\n\t" \
    "ds_read_b64_tr_b16 v[156:157], v166 offset:53248\n\t" \
    "ds_read_b64_tr_b16 v[158:159], v167 offset:53248\n\t" \
    "ds_read_b128 v[148:151], v160 offset:53760\n\t" \
    "s_waitcnt lgkmcnt(6)\n\t" \
    "v_mfma_f32_32x32x16_bf16 v[96:111], v[136:139], v[132:135], v[96:111]\n\t" \
    "v_mfma_f32_32x32x16_bf16 v[112:127], v[140:143], v[132:135], v[112:127]\n\t" \
    "s_waitcnt lgkmcnt(3)\n\t" \
    "v_mfma_f32_32x32x16_bf16 v[64:79], v[152:155], v[144:147], v[64:79]\n\t" \
    "ds_read_b128 v[128:131], v160 offset:57344\n\t" \
    "ds_read_b64_tr_b16 v[136:137], v164 offset:57344\n\t" \
    "ds_read_b64_tr_b16 v[138:139], v165 offset:57344\n\t" \
    "s_waitcnt lgkmcnt(4)\n\t" \
    "v_mfma_f32_32x32x16_bf16 v[80:95], v[156:159], v[144:147], v[80:95]\n\t" \
    "ds_read_b64_tr_b16 v[140:141], v166 offset:57344\n\t" \
    "ds_read_b64_tr_b16 v[142:143], v167 offset:57344\n\t" \
    "ds_read_b128 v[132:135], v160 offset:57856\n\t" \
    "s_waitcnt lgkmcnt(6)\n\t" \
    "v_mfma_f32_32x32x16_bf16 v[96:111], v[152:155], v[148:151], v[96:111]\n\t" \
    "v_mfma_f32_32x32x16_bf16 v[112:127], v[156:159], v[148:151], v[112:127]\n\t" \
    "s_waitcnt lgkmcnt(3)\n\t" \
    "v_mfma_f32_32x32x16_bf16 v[64:79], v[136:139], v[128:131], v[64:79]\n\t" \
    "ds_read_b128 v[144:147], v160 offset:61440\n\t" \
    "ds_read_b64_tr_b16 v[152:153], v164 offset:61440\n\t" \
    "ds_read_b64_tr_b16 v[154:155], v165 offset:61440\n\t" \
    "s_waitcnt lgkmcnt(4)\n\t" \
    "v_mfma_f32_32x32x16_bf16 v[80:95], v[140:143], v[128:131], v[80:95]\n\t" \
    "ds_read_b64_tr_b16 v[156:157], v166 offset:61440\n\t" \
    "ds_read_b64_tr_b16 v[158:159], v167 offset:61440\n\t" \
    "ds_read_b128 v[148:151], v160 offset:61952\n\t" \
    "s_waitcnt lgkmcnt(6)\n\t" \
    "v_mfma_f32_32x32x16_bf16 v[96:111], v[136:139], v[132:135], v[96:111]\n\t" \
    "v_mfma_f32_32x32x16_bf16 v[112:127], v[140:143], v[132:135], v[112:127]\n\t" \
    "s_waitcnt lgkmcnt(3)\n\t" \
    "v_mfma_f32_32x32x16_bf16 v[64:79], v[152:155], v[144:147], v[64:79]\n\t" \
    "s_waitcnt lgkmcnt(0)\n\t" \
    "s_barrier\n\t" \
    "ds_read_b128 v[128:131], v160 offset:0\n\t" \
    "ds_read_b64_tr_b16 v[136:137], v168 offset:0\n\t" \
    "ds_read_b64_tr_b16 v[138:139], v169 offset:0\n\t" \
    "v_mfma_f32_32x32x16_bf16 v[80:95], v[156:159], v[144:147], v[80:95]\n\t" \
    "ds_read_b64_tr_b16 v[140:141], v170 offset:0\n\t" \
    "ds_read_b64_tr_b16 v[142:143], v171 offset:0\n\t" \
    "ds_read_b128 v[132:135], v160 offset:512\n\t" \
    "v_mfma_f32_32x32x16_bf16 v[96:111], v[152:155], v[148:151], v[96:111]\n\t" \
    "v_mfma_f32_32x32x16_bf16 v[112:127], v[156:159], v[148:151], v[112:127]\n\t" \
    "s_waitcnt lgkmcnt(3)\n\t" \
    "v_mfma_f32_32x32x16_bf16 v[64:79], v[136:139], v[128:131], v[64:79]\n\t" \
    "ds_read_b128 v[144:147], v160 offset:4096\n\t" \
    "ds_read_b64_tr_b16 v[152:153], v168 offset:4096\n\t" \
    "ds_read_b64_tr_b16 v[154:155], v169 offset:4096\n\t" \
    "s_waitcnt lgkmcnt(4)\n\t" \
    "v_mfma_f32_32x32x16_bf16 v[80:95], v[140:143], v[128:131], v[80:95]\n\t" \
    "ds_read_b64_tr_b16 v[156:157], v170 offset:4096\n\t" \
    "ds_read_b64_tr_b16 v[158:159], v171 offset:4096\n\t" \
    "ds_read_b128 v[148:151], v160 offset:4608\n\t" \
    "s_waitcnt lgkmcnt(6)\n\t" \
    "v_mfma_f32_32x32x16_bf16 v[96:111], v[136:139], v[132:135], v[96:111]\n\t" \
    "v_mfma_f32_32x32x16_bf16 v[112:127], v[140:143], v[132:135], v[112:127]\n\t" \
    "s_waitcnt lgkmcnt(3)\n\t" \
    "v_mfma_f32_32x32x16_bf16 v[64:79], v[152:155], v[144:147], v[64:79]\n\t" \
    "ds_read_b128 v[128:131], v160 offset:8192\n\t" \
    "ds_read_b64_tr_b16 v[136:137], v168 offset:8192\n\t" \
    "ds_read_b64_tr_b16 v[138:139], v169 offset:8192\n\t" \
    "s_waitcnt lgkmcnt(4)\n\t" \
    "v_mfma_f32_32x32x16_bf16 v[80:95], v[156:159], v[144:147], v[80:95]\n\t" \
    "ds_read_b64_tr_b16 v[140:141], v170 offset:8192\n\t" \
    "ds_read_b64_tr_b16 v[142:143], v171 offset:8192\n\t" \
    "ds_read_b128 v[132:135], v160 offset:8704\n\t" \
    "s_waitcnt lgkmcnt(6)\n\t" \
    "v_mfma_f32_32x32x16_bf16 v[96:111], v[152:155], v[148:151], v[96:111]\n\t" \
    "v_mfma_f32_32x32x16_bf16 v[112:127], v[156:159], v[148:151], v[112:127]\n\t" \
    "s_waitcnt lgkmcnt(3)\n\t" \
    "v_mfma_f32_32x32x16_bf16 v[64:79], v[136:139], v[128:131], v[64:79]\n\t" \
    "ds_read_b128 v[144:147], v160 offset:12288\n\t" \
    "ds_read_b64_tr_b16 v[152:153], v168 offset:12288\n\t" \
    "ds_read_b64_tr_b16 v[154:155], v169 offset:12288\n\t" \
    "s_waitcnt lgkmcnt(4)\n\t" \
    "v_mfma_f32_32x32x16_bf16 v[80:95], v[140:143], v[128:131], v[80:95]\n\t" \
    "ds_read_b64_tr_b16 v[156:157], v170 offset:12288\n\t" \
    "ds_read_b64_tr_b16 v[158:159], v171 offset:12288\n\t" \
    "ds_read_b128 v[148:151], v160 offset:12800\n\t" \
    "s_waitcnt lgkmcnt(6)\n\t" \
    "v_mfma_f32_32x32x16_bf16 v[96:111], v[136:139], v[132:135], v[96:111]\n\t" \
    "v_mfma_f32_32x32x16_bf16 v[112:127], v[140:143], v[132:135], v[112:127]\n\t" \
    "s_waitcnt lgkmcnt(3)\n\t" \
    "v_mfma_f32_32x32x16_bf16 v[64:79], v[152:155], v[144:147], v[64:79]\n\t" \
    "s_waitcnt lgkmcnt(0)\n\t" \
    "s_barrier\n\t" \
    "ds_read_b128 v[128:131], v160 offset:16384\n\t" \
    "ds_read_b64_tr_b16 v[136:137], v164 offset:0\n\t" \
    "ds_read_b64_tr_b16 v[138:139], v165 offset:0\n\t" \
    "v_mfma_f32_32x32x16_bf16 v[80:95], v[156:159], v[144:147], v[80:95]\n\t" \
    "ds_read_b64_tr_b16 v[140:141], v166 offset:0\n\t" \
    "ds_read_b64_tr_b16 v[142:143], v167 offset:0\n\t" \
    "ds_read_b128 v[132:135], v160 offset:16896\n\t" \
    "v_mfma_f32_32x32x16_bf16 v[96:111], v[152:155], v[148:151], v[96:111]\n\t" \
    "v_mfma_f32_32x32x16_bf16 v[112:127], v[156:159], v[148:151], v[112:127]\n\t" \
    "s_waitcnt lgkmcnt(3)\n\t" \
    "v_mfma_f32_32x32x16_bf16 v[64:79], v[136:139], v[128:131], v[64:79]\n\t" \
    "ds_read_b128 v[144:147], v160 offset:20480\n\t" \
    "ds_read_b64_tr_b16 v[152:153], v164 offset:4096\n\t" \
    "ds_read_b64_tr_b16 v[154:155], v165 offset:4096\n\t" \
    "s_waitcnt lgkmcnt(4)\n\t" \
    "v_mfma_f32_32x32x16_bf16 v[80:95], v[140:143], v[128:131], v[80:95]\n\t" \
    "ds_read_b64_tr_b16 v[156:157], v166 offset:4096\n\t" \
    "ds_read_b64_tr_b16 v[158:159], v167 offset:4096\n\t" \
    "ds_read_b128 v[148:151], v160 offset:20992\n\t" \
    "s_waitcnt lgkmcnt(6)\n\t" \
    "v_mfma_f32_32x32x16_bf16 v[96:111], v[136:139], v[132:135], v[96:111]\n\t" \
    "v_mfma_f32_32x32x16_bf16 v[112:127], v[140:143], v[132:135], v[112:127]\n\t" \
    "s_waitcnt lgkmcnt(3)\n\t" \
    "v_mfma_f32_32x32x16_bf16 v[64:79], v[152:155], v[144:147], v[64:79]\n\t" \
    "ds_read_b128 v[128:131], v160 offset:24576\n\t" \
    "ds_read_b64_tr_b16 v[136:137], v164 offset:8192\n\t" \
    "ds_read_b64_tr_b16 v[138:139], v165 offset:8192\n\t" \
    "s_waitcnt lgkmcnt(4)\n\t" \
    "v_mfma_f32_32x32x16_bf16 v[80:95], v[156:159], v[144:147], v[80:95]\n\t" \
    "ds_read_b64_tr_b16 v[140:141], v166 offset:8192\n\t" \
    "ds_read_b64_tr_b16 v[142:143], v167 offset:8192\n\t" \
    "ds_read_b128 v[132:135], v160 offset:25088\n\t" \
    "s_waitcnt lgkmcnt(6)\n\t" \
    "v_mfma_f32_32x32x16_bf16 v[96:111], v[152:155], v[148:151], v[96:111]\n\t" \
    "v_mfma_f32_32x32x16_bf16 v[112:127], v[156:159], v[148:151], v[112:127]\n\t" \
    "s_waitcnt lgkmcnt(3)\n\t" \
    "v_mfma_f32_32x32x16_bf16 v[64:79], v[136:139], v[128:131], v[64:79]\n\t" \
    "ds_read_b128 v[144:147], v160 offset:28672\n\t" \
    "ds_read_b64_tr_b16 v[152:153], v164 offset:12288\n\t" \
    "ds_read_b64_tr_b16 v[154:155], v165 offset:12288\n\t" \
    "s_waitcnt lgkmcnt(4)\n\t" \
    "v_mfma_f32_32x32x16_bf16 v[80:95], v[140:143], v[128:131], v[80:95]\n\t" \
    "ds_read_b64_tr_b16 v[156:157], v166 offset:12288\n\t" \
    "ds_read_b64_tr_b16 v[158:159], v167 offset:12288\n\t" \
    "ds_read_b128 v[148:151], v160 offset:29184\n\t" \
    "s_waitcnt lgkmcnt(6)\n\t" \
    "v_mfma_f32_32x32x16_bf16 v[96:111], v[136:139], v[132:135], v[96:111]\n\t" \
    "v_mfma_f32_32x32x16_bf16 v[112:127], v[140:143], v[132:135], v[112:127]\n\t" \
    "s_waitcnt lgkmcnt(3)\n\t" \
    "v_mfma_f32_32x32x16_bf16 v[64:79], v[152:155], v[144:147], v[64:79]\n\t" \
    "s_waitcnt lgkmcnt(0)\n\t" \
    "s_barrier\n\t" \
    "ds_read_b128 v[128:131], v160 offset:32768\n\t" \
    "ds_read_b64_tr_b16 v[136:137], v164 offset:16384\n\t" \
    "ds_read_b64_tr_b16 v[138:139], v165 offset:16384\n\t" \
    "v_mfma_f32_32x32x16_bf16 v[80:95], v[156:159], v[144:147], v[80:95]\n\t" \
    "ds_read_b64_tr_b16 v[140:141], v166 offset:16384\n\t" \
    "ds_read_b64_tr_b16 v[142:143], v167 offset:16384\n\t" \
    "ds_read_b128 v[132:135], v160 offset:33280\n\t" \
    "v_mfma_f32_32x32x16_bf16 v[96:111], v[152:155], v[148:151], v[96:111]\n\t" \
    "v_mfma_f32_32x32x16_bf16 v[112:127], v[156:159], v[148:151], v[112:127]\n\t" \
    "s_waitcnt lgkmcnt(3)\n\t" \
    "v_mfma_f32_32x32x16_bf16 v[64:79], v[136:139], v[128:131], v[64:79]\n\t" \
    "ds_read_b128 v[144:147], v160 offset:36864\n\t" \
    "ds_read_b64_tr_b16 v[152:153], v164 offset:20480\n\t" \
    "ds_read_b64_tr_b16 v[154:155], v165 offset:20480\n\t" \
    "s_waitcnt lgkmcnt(4)\n\t" \
    "v_mfma_f32_32x32x16_bf16 v[80:95], v[140:143], v[128:131], v[80:95]\n\t" \
    "ds_read_b64_tr_b16 v[156:157], v166 offset:20480\n\t" \
    "ds_read_b64_tr_b16 v[158:159], v167 offset:20480\n\t" \
    "ds_read_b128 v[148:151], v160 offset:37376\n\t" \
    "s_waitcnt lgkmcnt(6)\n\t" \
    "v_mfma_f32_32x32x16_bf16 v[96:111], v[136:139], v[132:135], v[96:111]\n\t" \
    "v_mfma_f32_32x32x16_bf16 v[112:127], v[140:143], v[132:135], v[112:127]\n\t" \
    "s_waitcnt lgkmcnt(3)\n\t" \
    "v_mfma_f32_32x32x16_bf16 v[64:79], v[152:155], v[144:147], v[64:79]\n\t" \
    "ds_read_b128 v[128:131], v160 offset:40960\n\t" \
    "ds_read_b64_tr_b16 v[136:137], v164 offset:24576\n\t" \
    "ds_read_b64_tr_b16 v[138:139], v165 offset:24576\n\t" \
    "s_waitcnt lgkmcnt(4)\n\t" \
    "v_mfma_f32_32x32x16_bf16 v[80:95], v[156:159], v[144:147], v[80:95]\n\t" \
    "ds_read_b64_tr_b16 v[140:141], v166 offset:24576\n\t" \
    "ds_read_b64_tr_b16 v[142:143], v167 offset:24576\n\t" \
    "ds_read_b128 v[132:135], v160 offset:41472\n\t" \
    "s_waitcnt lgkmcnt(6)\n\t" \
    "v_mfma_f32_32x32x16_bf16 v[96:111], v[152:155], v[148:151], v[96:111]\n\t" \
    "v_mfma_f32_32x32x16_bf16 v[112:127], v[156:159], v[148:151], v[112:127]\n\t" \
    "s_waitcnt lgkmcnt(3)\n\t" \
    "v_mfma_f32_32x32x16_bf16 v[64:79], v[136:139], v[128:131], v[64:79]\n\t" \
    "ds_read_b128 v[144:147], v160 offset:45056\n\t" \
    "ds_read_b64_tr_b16 v[152:153], v164 offset:28672\n\t" \
    "ds_read_b64_tr_b16 v[154:155], v165 offset:28672\n\t" \
    "s_waitcnt lgkmcnt(4)\n\t" \
    "v_mfma_f32_32x32x16_bf16 v[80:95], v[140:143], v[128:131], v[80:95]\n\t" \
    "ds_read_b64_tr_b16 v[156:157], v166 offset:28672\n\t" \
    "ds_read_b64_tr_b16 v[158:159], v167 offset:28672\n\t" \
    "ds_read_b128 v[148:151], v160 offset:45568\n\t" \
    "s_waitcnt lgkmcnt(6)\n\t" \
    "v_mfma_f32_32x32x16_bf16 v[96:111], v[136:139], v[132:135], v[96:111]\n\t" \
    "v_mfma_f32_32x32x16_bf16 v[112:127], v[140:143], v[132:135], v[112:127]\n\t" \
    "s_waitcnt lgkmcnt(3)\n\t" \
    "v_mfma_f32_32x32x16_bf16 v[64:79], v[152:155], v[144:147], v[64:79]\n\t" \
    "s_waitcnt lgkmcnt(0)\n\t" \
    "s_barrier\n\t" \
    "ds_read_b128 v[128:131], v160 offset:49152\n\t" \
    "ds_read_b64_tr_b16 v[136:137], v164 offset:32768\n\t" \
    "ds_read_b64_tr_b16 v[138:139], v165 offset:32768\n\t" \
    "v_mfma_f32_32x32x16_bf16 v[80:95], v[156:159], v[144:147], v[80:95]\n\t" \
    "ds_read_b64_tr_b16 v[140:141], v166 offset:32768\n\t" \
    "ds_read_b64_tr_b16 v[142:143], v167 offset:32768\n\t" \
    "ds_read_b128 v[132:135], v160 offset:49664\n\t" \
    "v_mfma_f32_32x32x16_bf16 v[96:111], v[152:155], v[148:151], v[96:111]\n\t" \
    "v_mfma_f32_32x32x16_bf16 v[112:127], v[156:159], v[148:151], v[112:127]\n\t" \
    "s_waitcnt lgkmcnt(3)\n\t" \
    "v_mfma_f32_32x32x16_bf16 v[64:79], v[136:139], v[128:131], v[64:79]\n\t" \
    "ds_read_b128 v[144:147], v160 offset:53248\n\t" \
    "ds_read_b64_tr_b16 v[152:153], v164 offset:36864\n\t" \
    "ds_read_b64_tr_b16 v[154:155], v165 offset:36864\n\t" \
    "s_waitcnt lgkmcnt(4)\n\t" \
    "v_mfma_f32_32x32x16_bf16 v[80:95], v[140:143], v[128:131], v[80:95]\n\t" \
    "ds_read_b64_tr_b16 v[156:157], v166 offset:36864\n\t" \
    "ds_read_b64_tr_b16 v[158:159], v167 offset:36864\n\t" \
    "ds_read_b128 v[148:151], v160 offset:53760\n\t" \
    "s_waitcnt lgkmcnt(6)\n\t" \
    "v_mfma_f32_32x32x16_bf16 v[96:111], v[136:139], v[132:135], v[96:111]\n\t" \
    "v_mfma_f32_32x32x16_bf16 v[112:127], v[140:143], v[132:135], v[112:127]\n\t" \
    "s_waitcnt lgkmcnt(3)\n\t" \
    "v_mfma_f32_32x32x16_bf16 v[64:79], v[152:155], v[144:147], v[64:79]\n\t" \
    "ds_read_b128 v[128:131], v160 offset:57344\n\t" \
    "ds_read_b64_tr_b16 v[136:137], v164 offset:40960\n\t" \
    "ds_read_b64_tr_b16 v[138:139], v165 offset:40960\n\t" \
    "s_waitcnt lgkmcnt(4)\n\t" \
    "v_mfma_f32_32x32x16_bf16 v[80:95], v[156:159], v[144:147], v[80:95]\n\t" \
    "ds_read_b64_tr_b16 v[140:141], v166 offset:40960\n\t" \
    "ds_read_b64_tr_b16 v[142:143], v167 offset:40960\n\t" \
    "ds_read_b128 v[132:135], v160 offset:57856\n\t" \
    "s_waitcnt lgkmcnt(6)\n\t" \
    "v_mfma_f32_32x32x16_bf16 v[96:111], v[152:155], v[148:151], v[96:111]\n\t" \
    "v_mfma_f32_32x32x16_bf16 v[112:127], v[156:159], v[148:151], v[112:127]\n\t" \
    "s_waitcnt lgkmcnt(3)\n\t" \
    "v_mfma_f32_32x32x16_bf16 v[64:79], v[136:139], v[128:131], v[64:79]\n\t" \
    "ds_read_b128 v[144:147], v160 offset:61440\n\t" \
    "ds_read_b64_tr_b16 v[152:153], v164 offset:45056\n\t" \
    "ds_read_b64_tr_b16 v[154:155], v165 offset:45056\n\t" \
    "s_waitcnt lgkmcnt(4)\n\t" \
    "v_mfma_f32_32x32x16_bf16 v[80:95], v[140:143], v[128:131], v[80:95]\n\t" \
    "ds_read_b64_tr_b16 v[156:157], v166 offset:45056\n\t" \
    "ds_read_b64_tr_b16 v[158:159], v167 offset:45056\n\t" \
    "ds_read_b128 v[148:151], v160 offset:61952\n\t" \
    "s_waitcnt lgkmcnt(6)\n\t" \
    "v_mfma_f32_32x32x16_bf16 v[96:111], v[136:139], v[132:135], v[96:111]\n\t" \
    "v_mfma_f32_32x32x16_bf16 v[112:127], v[140:143], v[132:135], v[112:127]\n\t" \
    "s_waitcnt lgkmcnt(3)\n\t" \
    "v_mfma_f32_32x32x16_bf16 v[64:79], v[152:155], v[144:147], v[64:79]\n\t" \
    "s_waitcnt lgkmcnt(0)\n\t" \
    "s_barrier\n\t" \
    "ds_read_b128 v[128:131], v160 offset:0\n\t" \
    "ds_read_b64_tr_b16 v[136:137], v164 offset:49152\n\t" \
    "ds_read_b64_tr_b16 v[138:139], v165 offset:49152\n\t" \
    "v_mfma_f32_32x32x16_bf16 v[80:95], v[156:159], v[144:147], v[80:95]\n\t" \
    "ds_read_b64_tr_b16 v[140:141], v166 offset:49152\n\t" \
    "ds_read_b64_tr_b16 v[142:143], v167 offset:49152\n\t" \
    "ds_read_b128 v[132:135], v160 offset:512\n\t" \
    "v_mfma_f32_32x32x16_bf16 v[96:111], v[152:155], v[148:151], v[96:111]\n\t" \
    "v_mfma_f32_32x32x16_bf16 v[112:127], v[156:159], v[148:151], v[112:127]\n\t" \
    "s_waitcnt lgkmcnt(3)\n\t" \
    "v_mfma_f32_32x32x16_bf16 v[64:79], v[136:139], v[128:131], v[64:79]\n\t" \
    "ds_read_b128 v[144:147], v160 offset:4096\n\t" \
    "ds_read_b64_tr_b16 v[152:153], v164 offset:53248\n\t" \
    "ds_read_b64_tr_b16 v[154:155], v165 offset:53248\n\t" \
    "s_waitcnt lgkmcnt(4)\n\t" \
    "v_mfma_f32_32x32x16_bf16 v[80:95], v[140:143], v[128:131], v[80:95]\n\t" \
    "ds_read_b64_tr_b16 v[156:157], v166 offset:53248\n\t" \
    "ds_read_b64_tr_b16 v[158:159], v167 offset:53248\n\t" \
    "ds_read_b128 v[148:151], v160 offset:4608\n\t" \
    "s_waitcnt lgkmcnt(6)\n\t" \
    "v_mfma_f32_32x32x16_bf16 v[96:111], v[136:139], v[132:135], v[96:111]\n\t" \
    "v_mfma_f32_32x32x16_bf16 v[112:127], v[140:143], v[132:135], v[112:127]\n\t" \
    "s_waitcnt lgkmcnt(3)\n\t" \
    "v_mfma_f32_32x32x16_bf16 v[64:79], v[152:155], v[144:147], v[64:79]\n\t" \
    "ds_read_b128 v[128:131], v160 offset:8192\n\t" \
    "ds_read_b64_tr_b16 v[136:137], v164 offset:57344\n\t" \
    "ds_read_b64_tr_b16 v[138:139], v165 offset:57344\n\t" \
    "s_waitcnt lgkmcnt(4)\n\t" \
    "v_mfma_f32_32x32x16_bf16 v[80:95], v[156:159], v[144:147], v[80:95]\n\t" \
    "ds_read_b64_tr_b16 v[140:141], v166 offset:57344\n\t" \
    "ds_read_b64_tr_b16 v[142:143], v167 offset:57344\n\t" \
    "ds_read_b128 v[132:135], v160 offset:8704\n\t" \
    "s_waitcnt lgkmcnt(6)\n\t" \
    "v_mfma_f32_32x32x16_bf16 v[96:111], v[152:155], v[148:151], v[96:111]\n\t" \
    "v_mfma_f32_32x32x16_bf16 v[112:127], v[156:159], v[148:151], v[112:127]\n\t" \
    "s_waitcnt lgkmcnt(3)\n\t" \
    "v_mfma_f32_32x32x16_bf16 v[64:79], v[136:139], v[128:131], v[64:79]\n\t" \
    "ds_read_b128 v[144:147], v160 offset:12288\n\t" \
    "ds_read_b64_tr_b16 v[152:153], v164 offset:61440\n\t" \
    "ds_read_b64_tr_b16 v[154:155], v165 offset:61440\n\t" \
    "s_waitcnt lgkmcnt(4)\n\t" \
    "v_mfma_f32_32x32x16_bf16 v[80:95], v[140:143], v[128:131], v[80:95]\n\t" \
    "ds_read_b64_tr_b16 v[156:157], v166 offset:61440\n\t" \
    "ds_read_b64_tr_b16 v[158:159], v167 offset:61440\n\t" \
    "ds_read_b128 v[148:151], v160 offset:12800\n\t" \
    "s_waitcnt lgkmcnt(6)\n\t" \
    "v_mfma_f32_32x32x16_bf16 v[96:111], v[136:139], v[132:135], v[96:111]\n\t" \
    "v_mfma_f32_32x32x16_bf16 v[112:127], v[140:143], v[132:135], v[112:127]\n\t" \
    "s_waitcnt lgkmcnt(3)\n\t" \
    "v_mfma_f32_32x32x16_bf16 v[64:79], v[152:155], v[144:147], v[64:79]\n\t" \
    "s_waitcnt lgkmcnt(0)\n\t" \
    "s_barrier\n\t" \
    "ds_read_b128 v[128:131], v160 offset:16384\n\t" \
    "ds_read_b64_tr_b16 v[136:137], v168 offset:0\n\t" \
    "ds_read_b64_tr_b16 v[138:139], v169 offset:0\n\t" \
    "v_mfma_f32_32x32x16_bf16 v[80:95], v[156:159], v[144:147], v[80:95]\n\t" \
    "ds_read_b64_tr_b16 v[140:141], v170 offset:0\n\t" \
    "ds_read_b64_tr_b16 v[142:143], v171 offset:0\n\t" \
    "ds_read_b128 v[132:135], v160 offset:16896\n\t" \
    "v_mfma_f32_32x32x16_bf16 v[96:111], v[152:155], v[148:151], v[96:111]\n\t" \
    "v_mfma_f32_32x32x16_bf16 v[112:127], v[156:159], v[148:151], v[112:127]\n\t" \
    "s_waitcnt lgkmcnt(3)\n\t" \
    "v_mfma_f32_32x32x16_bf16 v[64:79], v[136:139], v[128:131], v[64:79]\n\t" \
    "ds_read_b128 v[144:147], v160 offset:20480\n\t" \
    "ds_read_b64_tr_b16 v[152:153], v168 offset:4096\n\t" \
    "ds_read_b64_tr_b16 v[154:155], v169 offset:4096\n\t" \
    "s_waitcnt lgkmcnt(4)\n\t" \
    "v_mfma_f32_32x32x16_bf16 v[80:95], v[140:143], v[128:131], v[80:95]\n\t" \
    "ds_read_b64_tr_b16 v[156:157], v170 offset:4096\n\t" \
    "ds_read_b64_tr_b16 v[158:159], v171 offset:4096\n\t" \
    "ds_read_b128 v[148:151], v160 offset:20992\n\t" \
    "s_waitcnt lgkmcnt(6)\n\t" \
    "v_mfma_f32_32x32x16_bf16 v[96:111], v[136:139], v[132:135], v[96:111]\n\t" \
    "v_mfma_f32_32x32x16_bf16 v[112:127], v[140:143], v[132:135], v[112:127]\n\t" \
    "s_waitcnt lgkmcnt(3)\n\t" \
    "v_mfma_f32_32x32x16_bf16 v[64:79], v[152:155], v[144:147], v[64:79]\n\t" \
    "ds_read_b128 v[128:131], v160 offset:24576\n\t" \
    "ds_read_b64_tr_b16 v[136:137], v168 offset:8192\n\t" \
    "ds_read_b64_tr_b16 v[138:139], v169 offset:8192\n\t" \
    "s_waitcnt lgkmcnt(4)\n\t" \
    "v_mfma_f32_32x32x16_bf16 v[80:95], v[156:159], v[144:147], v[80:95]\n\t" \
    "ds_read_b64_tr_b16 v[140:141], v170 offset:8192\n\t" \
    "ds_read_b64_tr_b16 v[142:143], v171 offset:8192\n\t" \
    "ds_read_b128 v[132:135], v160 offset:25088\n\t" \
    "s_waitcnt lgkmcnt(6)\n\t" \
    "v_mfma_f32_32x32x16_bf16 v[96:111], v[152:155], v[148:151], v[96:111]\n\t" \
    "v_mfma_f32_32x32x16_bf16 v[112:127], v[156:159], v[148:151], v[112:127]\n\t" \
    "s_waitcnt lgkmcnt(3)\n\t" \
    "v_mfma_f32_32x32x16_bf16 v[64:79], v[136:139], v[128:131], v[64:79]\n\t" \
    "ds_read_b128 v[144:147], v160 offset:28672\n\t" \
    "ds_read_b64_tr_b16 v[152:153], v168 offset:12288\n\t" \
    "ds_read_b64_tr_b16 v[154:155], v169 offset:12288\n\t" \
    "s_waitcnt lgkmcnt(4)\n\t" \
    "v_mfma_f32_32x32x16_bf16 v[80:95], v[140:143], v[128:131], v[80:95]\n\t" \
    "ds_read_b64_tr_b16 v[156:157], v170 offset:12288\n\t" \
    "ds_read_b64_tr_b16 v[158:159], v171 offset:12288\n\t" \
    "ds_read_b128 v[148:151], v160 offset:29184\n\t" \
    "s_waitcnt lgkmcnt(6)\n\t" \
    "v_mfma_f32_32x32x16_bf16 v[96:111], v[136:139], v[132:135], v[96:111]\n\t" \
    "v_mfma_f32_32x32x16_bf16 v[112:127], v[140:143], v[132:135], v[112:127]\n\t" \
    "s_waitcnt lgkmcnt(3)\n\t" \
    "v_mfma_f32_32x32x16_bf16 v[64:79], v[152:155], v[144:147], v[64:79]\n\t" \
    "s_waitcnt lgkmcnt(0)\n\t" \
    "v_mfma_f32_32x32x16_bf16 v[80:95], v[156:159], v[144:147], v[80:95]\n\t" \
    "v_mfma_f32_32x32x16_bf16 v[96:111], v[152:155], v[148:151], v[96:111]\n\t" \
    "v_mfma_f32_32x32x16_bf16 v[112:127], v[156:159], v[148:151], v[112:127]\n\t" \
    "s_nop 15\n\t" \
    ""

template <int MODE> struct T1Tile {
    int rb, cb; bool active;
    __device__ __forceinline__ T1Tile() {
        const int xcd = blockIdx.x & 7, j = blockIdx.x >> 3;
        rb = (xcd >> 1) * 4 + (j & 3);
        cb = MODE == 0 ? (xcd & 1) * 8 + (j >> 2) : (xcd & 1) * 7 + (j >> 2);
        active = MODE == 0 ? cb < 15 : (j >> 2) < 7;
    }
};
template <int MODE> struct T1Loader {
    static constexpr int N = MODE == 0 ? 1920 : F_, LDB = N * 2;
    rsrc_t r; unsigned v[4], s0, l0; bool isB;
    __device__ __forceinline__ T1Loader(const Args& a, unsigned lds0, int w, int lane, int rb, int cb) {
        isB = w >= 6;
        const int i = w & 1;
        if (!isB) {
            if (MODE == 0) {
                r = mk_rsrc(a.x, S_ * D_ * 2);
#pragma unroll
                for (int p = 0; p < 2; ++p) v[p] = (64 * i + (lane >> 3)) * (D_ * 2) + (((lane & 7) ^ (4 * p + (lane >> 4))) * 16);
                s0 = rb * 128 * (D_ * 2);
            } else {
                r = mk_rsrc(a.ws + WS_H, S_ * D_ * 2);
#pragma unroll
                for (int p = 0; p < 2; ++p) v[p] = (2 * p + (lane >> 5)) * 40960 + (lane & 31) * 16;
                s0 = rb * 4 * 40960 + i * 4 * 512;
            }
            v[2] = v[3] = 0;
            l0 = lds0 + T1_A + i * 8192;
        } else {
            r = MODE == 0 ? mk_rsrc(a.wqkv, D_ * N * 2) : mk_rsrc(a.w1, D_ * N * 2);
#pragma unroll
            for (int p = 0; p < 4; ++p) v[p] = (32 * i + (lane >> 4)) * LDB + (((lane & 15) ^ ((lane >> 4) << 2) ^ p) * 16);
            s0 = cb * 256; l0 = lds0 + T1_B + i * 8192;
        }
    }
    __device__ __forceinline__ void issueA(int c) const {
#pragma unroll
        for (int p = 0; p < 8; ++p)
            dma16<MODE == 1>(l0 + (c % T1_NA) * 16384 + p * 1024, v[p & 1], r, MODE == 0 ? s0 + p * 8 * (D_ * 2) + c * 128 : s0 + (8 * c + (p >> 1)) * 512);
    }
    __device__ __forceinline__ void issueB(int c) const {
#pragma unroll
        for (int p = 0; p < 8; ++p) dma16<false>(l0 + (c % T1_NB) * 16384 + p * 1024, v[p & 3], r, s0 + (c * 64 + 4 * p) * LDB);
    }
    __device__ __forceinline__ void prologueB() const {
        if (isB) {
#pragma unroll
            for (int c = 0; c < T1_NB; ++c) issueB(c);
        }
    }
    template <class H, class G> __device__ __forceinline__ void run(H&& hook, G&& after_prologue) const {
        if (!isB) {
#pragma unroll
            for (int c = 0; c < T1_NA; ++c) issueA(c);
            after_prologue();
            ring_steps<10, T1_NA, 8, 0, 0>([&](int c) { issueA(c); }, hook);
        } else {
            if (MODE == 1) WAIT_VM(0);
            after_prologue();
            ring_steps<10, T1_NB, 8, 0, 0>([&](int c) { issueB(c); }, hook);
        }
    }
};

__device__ __forceinline__ void w1_preload(const Args& a, unsigned lds0, int lw, int lane, int cb) {
    constexpr int LDB = F_ * 2;
    const rsrc_t r = mk_rsrc(a.w1, D_ * LDB);
#pragma unroll
    for (int p = 0; p < 4; ++p) {
        const unsigned v = (lane >> 4) * LDB + (((lane & 15) ^ ((lane >> 4) << 2) ^ p) * 16);
#pragma unroll
        for (int c = 0; c < T1_NB; ++c) dma16<false>(lds0 + T1_B + c * 16384 + (4 * lw + p) * 1024, v, r, cb * 256 + (c * 64 + 4 * (4 * lw + p)) * LDB);
    }
}

template <int MODE>
__device__ __forceinline__ void t1_consume(unsigned lds0, int w, int lane, bool vblk, f32x16 (&acc)[2][2], rsrc_t rws, unsigned goff, u32x4& gran) {
    const int wr = w >> 1, wc = w & 1, r32 = lane & 31, hi = lane >> 5;
    unsigned aA[4], aB[2][2];
#pragma unroll
    for (int s = 0; s < 4; ++s)
        aA[s] = MODE == 0 ? lds0 + T1_A + (64 * wr + r32) * 128 + (((2 * s + hi) ^ ((r32 >> 1) & 7)) * 16)
                          : lds0 + T1_A + hi * 2048 + (64 * wr + r32) * 16;
    const int g = lane >> 4, i = lane & 15, q = i >> 2, p = i & 3;
#pragma unroll
    for (int u = 0; u < 2; ++u)
#pragma unroll
        for (int jj = 0; jj < 2; ++jj) {
            const int k = 8 * (g >> 1) + 4 * jj + q, ch = 8 * wc + 4 * u + 2 * (g & 1) + (p >> 1);
            const int swz = (q << 2) | (2 * (g >> 1) + jj);
            aB[u][jj] = lds0 + T1_B + k * 256 + ((ch ^ swz) * 16) + 8 * (p & 1);
        }
    f32x16 c00 = {}, c01 = {}, c10 = {}, c11 = {};
#define T1_OUT "+{v[64:79]}"(c00), "+{v[80:95]}"(c01), "+{v[96:111]}"(c10), "+{v[112:127]}"(c11)
#define T1_IN "{v160}"(aA[0]), "{v161}"(aA[1]), "{v162}"(aA[2]), "{v163}"(aA[3]), \
          "{v164}"(aB[0][0]), "{v165}"(aB[0][1]), "{v166}"(aB[1][0]), "{v167}"(aB[1][1]), \
          "{v168}"(aB[0][0] + 65536), "{v169}"(aB[0][1] + 65536), "{v170}"(aB[1][0] + 65536), "{v171}"(aB[1][1] + 65536)
#define T1_CLOB "memory", "v128", "v129", "v130", "v131", "v132", "v133", "v134", "v135", "v136", "v137", "v138", "v139", "v140", "v141", "v142", "v143", \
          "v144", "v145", "v146", "v147", "v148", "v149", "v150", "v151", "v152", "v153", "v154", "v155", "v156", "v157", "v158", "v159"
    if (MODE == 1)  asm volatile(T1_LOOP_ASM_F : T1_OUT : T1_IN : T1_CLOB);
    else if (vblk)  asm volatile(T1_LOOP_ASM_V : T1_OUT, "=&{v[172:175]}"(gran) : T1_IN, "{v176}"(goff), [rws] "s"(rws) : T1_CLOB);
    else            asm volatile(T1_LOOP_ASM_QK : T1_OUT, "=&{v[172:175]}"(gran) : T1_IN, "{v176}"(goff), [rws] "s"(rws) : T1_CLOB);
#undef T1_OUT
#undef T1_IN
#undef T1_CLOB
    acc[0][0] = c00; acc[0][1] = c01; acc[1][0] = c10; acc[1][1] = c11;
}

#define T2_LOOP_ASM_10 \
    "s_barrier\n\t" \
    "ds_read_b128 v[104:107], v160 offset:0\n\t" \
    "ds_read_b128 v[112:115], v162 offset:0\n\t" \
    "ds_read_b128 v[108:111], v161 offset:0\n\t" \
    "ds_read_b128 v[116:119], v162 offset:1024\n\t" \
    "ds_read_b128 v[120:123], v162 offset:2048\n\t" \
    "ds_read_b128 v[124:127], v162 offset:3072\n\t" \
    "ds_read_b128 v[128:131], v162 offset:4096\n\t" \
    "s_barrier\n\t" \
    "ds_read_b128 v[132:135], v160 offset:8192\n\t" \
    "ds_read_b128 v[140:143], v162 offset:10240\n\t" \
    "ds_read_b128 v[136:139], v161 offset:8192\n\t" \
    "ds_read_b128 v[144:147], v162 offset:11264\n\t" \
    "ds_read_b128 v[148:151], v162 offset:12288\n\t" \
    "ds_read_b128 v[152:155], v162 offset:13312\n\t" \
    "ds_read_b128 v[156:159], v162 offset:14336\n\t" \
    "s_waitcnt lgkmcnt(7)\n\t" \
    "s_barrier\n\t" \
    "v_mfma_f32_16x16x32_bf16 v[64:67], v[112:115], v[104:107], v[64:67]\n\t" \
    "ds_read_b128 v[166:169], v160 offset:16384\n\t" \
    "v_mfma_f32_16x16x32_bf16 v[84:87], v[112:115], v[108:111], v[84:87]\n\t" \
    "ds_read_b128 v[174:177], v162 offset:20480\n\t" \
    "v_mfma_f32_16x16x32_bf16 v[68:71], v[116:119], v[104:107], v[68:71]\n\t" \
    "ds_read_b128 v[170:173], v161 offset:16384\n\t" \
    "v_mfma_f32_16x16x32_bf16 v[88:91], v[116:119], v[108:111], v[88:91]\n\t" \
    "ds_read_b128 v[178:181], v162 offset:21504\n\t" \
    "v_mfma_f32_16x16x32_bf16 v[72:75], v[120:123], v[104:107], v[72:75]\n\t" \
    "ds_read_b128 v[182:185], v162 offset:22528\n\t" \
    "v_mfma_f32_16x16x32_bf16 v[92:95], v[120:123], v[108:111], v[92:95]\n\t" \
    "ds_read_b128 v[186:189], v162 offset:23552\n\t" \
    "v_mfma_f32_16x16x32_bf16 v[76:79], v[124:127], v[104:107], v[76:79]\n\t" \
    "ds_read_b128 v[190:193], v162 offset:24576\n\t" \
    "v_mfma_f32_16x16x32_bf16 v[96:99], v[124:127], v[108:111], v[96:99]\n\t" \
    "v_mfma_f32_16x16x32_bf16 v[80:83], v[128:131], v[104:107], v[80:83]\n\t" \
    "v_mfma_f32_16x16x32_bf16 v[100:103], v[128:131], v[108:111], v[100:103]\n\t" \
    "s_waitcnt lgkmcnt(7)\n\t" \
    "s_barrier\n\t" \
    "v_mfma_f32_16x16x32_bf16 v[64:67], v[140:143], v[132:135], v[64:67]\n\t" \
    "ds_read_b128 v[104:107], v160 offset:24576\n\t" \
    "v_mfma_f32_16x16x32_bf16 v[84:87], v[140:143], v[136:139], v[84:87]\n\t" \
    "ds_read_b128 v[112:115], v162 offset:30720\n\t" \
    "v_mfma_f32_16x16x32_bf16 v[68:71], v[144:147], v[132:135], v[68:71]\n\t" \
    "ds_read_b128 v[108:111], v161 offset:24576\n\t" \
    "v_mfma_f32_16x16x32_bf16 v[88:91], v[144:147], v[136:139], v[88:91]\n\t" \
    "ds_read_b128 v[116:119], v162 offset:31744\n\t" \
    "v_mfma_f32_16x16x32_bf16 v[72:75], v[148:151], v[132:135], v[72:75]\n\t" \
    "ds_read_b128 v[120:123], v162 offset:32768\n\t" \
    "v_mfma_f32_16x16x32_bf16 v[92:95], v[148:151], v[136:139], v[92:95]\n\t" \
    "ds_read_b128 v[124:127], v162 offset:33792\n\t" \
    "v_mfma_f32_16x16x32_bf16 v[76:79], v[152:155], v[132:135], v[76:79]\n\t" \
    "ds_read_b128 v[128:131], v162 offset:34816\n\t" \
    "v_mfma_f32_16x16x32_bf16 v[96:99], v[152:155], v[136:139], v[96:99]\n\t" \
    "v_mfma_f32_16x16x32_bf16 v[80:83], v[156:159], v[132:135], v[80:83]\n\t" \
    "v_mfma_f32_16x16x32_bf16 v[100:103], v[156:159], v[136:139], v[100:103]\n\t" \
    "s_waitcnt lgkmcnt(7)\n\t" \
    "s_barrier\n\t" \
    "v_mfma_f32_16x16x32_bf16 v[64:67], v[174:177], v[166:169], v[64:67]\n\t" \
    "ds_read_b128 v[132:135], v160 offset:32768\n\t" \
    "v_mfma_f32_16x16x32_bf16 v[84:87], v[174:177], v[170:173], v[84:87]\n\t" \
    "ds_read_b128 v[140:143], v162 offset:40960\n\t" \
    "v_mfma_f32_16x16x32_bf16 v[68:71], v[178:181], v[166:169], v[68:71]\n\t" \
    "ds_read_b128 v[136:139], v161 offset:32768\n\t" \
    "v_mfma_f32_16x16x32_bf16 v[88:91], v[178:181], v[170:173], v[88:91]\n\t" \
    "ds_read_b128 v[144:147], v162 offset:41984\n\t" \
    "v_mfma_f32_16x16x32_bf16 v[72:75], v[182:185], v[166:169], v[72:75]\n\t" \
    "ds_read_b128 v[148:151], v162 offset:43008\n\t" \
    "v_mfma_f32_16x16x32_bf16 v[92:95], v[182:185], v[170:173], v[92:95]\n\t" \
    "ds_read_b128 v[152:155], v162 offset:44032\n\t" \
    "v_mfma_f32_16x16x32_bf16 v[76:79], v[186:189], v[166:169], v[76:79]\n\t" \
    "ds_read_b128 v[156:159], v162 offset:45056\n\t" \
    "v_mfma_f32_16x16x32_bf16 v[96:99], v[186:189], v[170:173], v[96:99]\n\t" \
    "v_mfma_f32_16x16x32_bf16 v[80:83], v[190:193], v[166:169], v[80:83]\n\t" \
    "v_mfma_f32_16x16x32_bf16 v[100:103], v[190:193], v[170:173], v[100:103]\n\t" \
    "s_waitcnt lgkmcnt(7)\n\t" \
    "s_barrier\n\t" \
    "v_mfma_f32_16x16x32_bf16 v[64:67], v[112:115], v[104:107], v[64:67]\n\t" \
    "ds_read_b128 v[166:169], v160 offset:40960\n\t" \
    "v_mfma_f32_16x16x32_bf16 v[84:87], v[112:115], v[108:111], v[84:87]\n\t" \
    "ds_read_b128 v[174:177], v162 offset:51200\n\t" \
    "v_mfma_f32_16x16x32_bf16 v[68:71], v[116:119], v[104:107], v[68:71]\n\t" \
    "ds_read_b128 v[170:173], v161 offset:40960\n\t" \
    "v_mfma_f32_16x16x32_bf16 v[88:91], v[116:119], v[108:111], v[88:91]\n\t" \
    "ds_read_b128 v[178:181], v162 offset:52224\n\t" \
    "v_mfma_f32_16x16x32_bf16 v[72:75], v[120:123], v[104:107], v[72:75]\n\t" \
    "ds_read_b128 v[182:185], v162 offset:53248\n\t" \
    "v_mfma_f32_16x16x32_bf16 v[92:95], v[120:123], v[108:111], v[92:95]\n\t" \
    "ds_read_b128 v[186:189], v162 offset:54272\n\t" \
    "v_mfma_f32_16x16x32_bf16 v[76:79], v[124:127], v[104:107], v[76:79]\n\t" \
    "ds_read_b128 v[190:193], v162 offset:55296\n\t" \
    "v_mfma_f32_16x16x32_bf16 v[96:99], v[124:127], v[108:111], v[96:99]\n\t" \
    "v_mfma_f32_16x16x32_bf16 v[80:83], v[128:131], v[104:107], v[80:83]\n\t" \
    "v_mfma_f32_16x16x32_bf16 v[100:103], v[128:131], v[108:111], v[100:103]\n\t" \
    "s_waitcnt lgkmcnt(7)\n\t" \
    "s_barrier\n\t" \
    "v_mfma_f32_16x16x32_bf16 v[64:67], v[140:143], v[132:135], v[64:67]\n\t" \
    "ds_read_b128 v[104:107], v160 offset:0\n\t" \
    "v_mfma_f32_16x16x32_bf16 v[84:87], v[140:143], v[136:139], v[84:87]\n\t" \
    "ds_read_b128 v[112:115], v163 offset:0\n\t" \
    "v_mfma_f32_16x16x32_bf16 v[68:71], v[144:147], v[132:135], v[68:71]\n\t" \
    "ds_read_b128 v[108:111], v161 offset:0\n\t" \
    "v_mfma_f32_16x16x32_bf16 v[88:91], v[144:147], v[136:139], v[88:91]\n\t" \
    "ds_read_b128 v[116:119], v163 offset:1024\n\t" \
    "v_mfma_f32_16x16x32_bf16 v[72:75], v[148:151], v[132:135], v[72:75]\n\t" \
    "ds_read_b128 v[120:123], v163 offset:2048\n\t" \
    "v_mfma_f32_16x16x32_bf16 v[92:95], v[148:151], v[136:139], v[92:95]\n\t" \
    "ds_read_b128 v[124:127], v163 offset:3072\n\t" \
    "v_mfma_f32_16x16x32_bf16 v[76:79], v[152:155], v[132:135], v[76:79]\n\t" \
    "ds_read_b128 v[128:131], v163 offset:4096\n\t" \
    "v_mfma_f32_16x16x32_bf16 v[96:99], v[152:155], v[136:139], v[96:99]\n\t" \
    "v_mfma_f32_16x16x32_bf16 v[80:83], v[156:159], v[132:135], v[80:83]\n\t" \
    "v_mfma_f32_16x16x32_bf16 v[100:103], v[156:159], v[136:139], v[100:103]\n\t" \
    "s_waitcnt lgkmcnt(7)\n\t" \
    "s_barrier\n\t" \
    "v_mfma_f32_16x16x32_bf16 v[64:67], v[174:177], v[166:169], v[64:67]\n\t" \
    "ds_read_b128 v[132:135], v160 offset:8192\n\t" \
    "v_mfma_f32_16x16x32_bf16 v[84:87], v[174:177], v[170:173], v[84:87]\n\t" \
    "ds_read_b128 v[140:143], v163 offset:10240\n\t" \
    "v_mfma_f32_16x16x32_bf16 v[68:71], v[178:181], v[166:169], v[68:71]\n\t" \
    "ds_read_b128 v[136:139], v161 offset:8192\n\t" \
    "v_mfma_f32_16x16x32_bf16 v[88:91], v[178:181], v[170:173], v[88:91]\n\t" \
    "ds_read_b128 v[144:147], v163 offset:11264\n\t" \
    "v_mfma_f32_16x16x32_bf16 v[72:75], v[182:185], v[166:169], v[72:75]\n\t" \
    "ds_read_b128 v[148:151], v163 offset:12288\n\t" \
    "v_mfma_f32_16x16x32_bf16 v[92:95], v[182:185], v[170:173], v[92:95]\n\t" \
    "ds_read_b128 v[152:155], v163 offset:13312\n\t" \
    "v_mfma_f32_16x16x32_bf16 v[76:79], v[186:189], v[166:169], v[76:79]\n\t" \
    "ds_read_b128 v[156:159], v163 offset:14336\n\t" \
    "v_mfma_f32_16x16x32_bf16 v[96:99], v[186:189], v[170:173], v[96:99]\n\t" \
    "v_mfma_f32_16x16x32_bf16 v[80:83], v[190:193], v[166:169], v[80:83]\n\t" \
    "v_mfma_f32_16x16x32_bf16 v[100:103], v[190:193], v[170:173], v[100:103]\n\t" \
    "s_waitcnt lgkmcnt(7)\n\t" \
    "s_barrier\n\t" \
    "v_mfma_f32_16x16x32_bf16 v[64:67], v[112:115], v[104:107], v[64:67]\n\t" \
    "ds_read_b128 v[166:169], v160 offset:16384\n\t" \
    "v_mfma_f32_16x16x32_bf16 v[84:87], v[112:115], v[108:111], v[84:87]\n\t" \
    "ds_read_b128 v[174:177], v163 offset:20480\n\t" \
    "v_mfma_f32_16x16x32_bf16 v[68:71], v[116:119], v[104:107], v[68:71]\n\t" \
    "ds_read_b128 v[170:173], v161 offset:16384\n\t" \
    "v_mfma_f32_16x16x32_bf16 v[88:91], v[116:119], v[108:111], v[88:91]\n\t" \
    "ds_read_b128 v[178:181], v163 offset:21504\n\t" \
    "v_mfma_f32_16x16x32_bf16 v[72:75], v[120:123], v[104:107], v[72:75]\n\t" \
    "ds_read_b128 v[182:185], v163 offset:22528\n\t" \
    "v_mfma_f32_16x16x32_bf16 v[92:95], v[120:123], v[108:111], v[92:95]\n\t" \
    "ds_read_b128 v[186:189], v163 offset:23552\n\t" \
    "v_mfma_f32_16x16x32_bf16 v[76:79], v[124:127], v[104:107], v[76:79]\n\t" \
    "ds_read_b128 v[190:193], v163 offset:24576\n\t" \
    "v_mfma_f32_16x16x32_bf16 v[96:99], v[124:127], v[108:111], v[96:99]\n\t" \
    "v_mfma_f32_16x16x32_bf16 v[80:83], v[128:131], v[104:107], v[80:83]\n\t" \
    "v_mfma_f32_16x16x32_bf16 v[100:103], v[128:131], v[108:111], v[100:103]\n\t" \
    "s_waitcnt lgkmcnt(7)\n\t" \
    "s_barrier\n\t" \
    "v_mfma_f32_16x16x32_bf16 v[64:67], v[140:143], v[132:135], v[64:67]\n\t" \
    "ds_read_b128 v[104:107], v160 offset:24576\n\t" \
    "v_mfma_f32_16x16x32_bf16 v[84:87], v[140:143], v[136:139], v[84:87]\n\t" \
    "ds_read_b128 v[112:115], v163 offset:30720\n\t" \
    "v_mfma_f32_16x16x32_bf16 v[68:71], v[144:147], v[132:135], v[68:71]\n\t" \
    "ds_read_b128 v[108:111], v161 offset:24576\n\t" \
    "v_mfma_f32_16x16x32_bf16 v[88:91], v[144:147], v[136:139], v[88:91]\n\t" \
    "ds_read_b128 v[116:119], v163 offset:31744\n\t" \
    "v_mfma_f32_16x16x32_bf16 v[72:75], v[148:151], v[132:135], v[72:75]\n\t" \
    "ds_read_b128 v[120:123], v163 offset:32768\n\t" \
    "v_mfma_f32_16x16x32_bf16 v[92:95], v[148:151], v[136:139], v[92:95]\n\t" \
    "ds_read_b128 v[124:127], v163 offset:33792\n\t" \
    "v_mfma_f32_16x16x32_bf16 v[76:79], v[152:155], v[132:135], v[76:79]\n\t" \
    "ds_read_b128 v[128:131], v163 offset:34816\n\t" \
    "v_mfma_f32_16x16x32_bf16 v[96:99], v[152:155], v[136:139], v[96:99]\n\t" \
    "v_mfma_f32_16x16x32_bf16 v[80:83], v[156:159], v[132:135], v[80:83]\n\t" \
    "v_mfma_f32_16x16x32_bf16 v[100:103], v[156:159], v[136:139], v[100:103]\n\t" \
    "s_waitcnt lgkmcnt(7)\n\t" \
    "v_mfma_f32_16x16x32_bf16 v[64:67], v[174:177], v[166:169], v[64:67]\n\t" \
    "v_mfma_f32_16x16x32_bf16 v[84:87], v[174:177], v[170:173], v[84:87]\n\t" \
    "v_mfma_f32_16x16x32_bf16 v[68:71], v[178:181], v[166:169], v[68:71]\n\t" \
    "v_mfma_f32_16x16x32_bf16 v[88:91], v[178:181], v[170:173], v[88:91]\n\t" \
    "v_mfma_f32_16x16x32_bf16 v[72:75], v[182:185], v[166:169], v[72:75]\n\t" \
    "v_mfma_f32_16x16x32_bf16 v[92:95], v[182:185], v[170:173], v[92:95]\n\t" \
    "v_mfma_f32_16x16x32_bf16 v[76:79], v[186:189], v[166:169], v[76:79]\n\t" \
    "v_mfma_f32_16x16x32_bf16 v[96:99], v[186:189], v[170:173], v[96:99]\n\t" \
    "v_mfma_f32_16x16x32_bf16 v[80:83], v[190:193], v[166:169], v[80:83]\n\t" \
    "v_mfma_f32_16x16x32_bf16 v[100:103], v[190:193], v[170:173], v[100:103]\n\t" \
    "s_waitcnt lgkmcnt(0)\n\t" \
    "v_mfma_f32_16x16x32_bf16 v[64:67], v[112:115], v[104:107], v[64:67]\n\t" \
    "v_mfma_f32_16x16x32_bf16 v[84:87], v[112:115], v[108:111], v[84:87]\n\t" \
    "v_mfma_f32_16x16x32_bf16 v[68:71], v[116:119], v[104:107], v[68:71]\n\t" \
    "v_mfma_f32_16x16x32_bf16 v[88:91], v[116:119], v[108:111], v[88:91]\n\t" \
    "v_mfma_f32_16x16x32_bf16 v[72:75], v[120:123], v[104:107], v[72:75]\n\t" \
    "v_mfma_f32_16x16x32_bf16 v[92:95], v[120:123], v[108:111], v[92:95]\n\t" \
    "v_mfma_f32_16x16x32_bf16 v[76:79], v[124:127], v[104:107], v[76:79]\n\t" \
    "v_mfma_f32_16x16x32_bf16 v[96:99], v[124:127], v[108:111], v[96:99]\n\t" \
    "v_mfma_f32_16x16x32_bf16 v[80:83], v[128:131], v[104:107], v[80:83]\n\t" \
    "v_mfma_f32_16x16x32_bf16 v[100:103], v[128:131], v[108:111], v[100:103]\n\t" \
    "s_nop 15\n\t" \
    ""
#define T2_LOOP_ASM_27 \
    "s_barrier\n\t" \
    "ds_read_b128 v[104:107], v160 offset:0\n\t" \
    "ds_read_b128 v[112:115], v162 offset:0\n\t" \
    "ds_read_b128 v[108:111], v161 offset:0\n\t" \
    "ds_read_b128 v[116:119], v162 offset:1024\n\t" \
    "ds_read_b128 v[120:123], v162 offset:2048\n\t" \
    "ds_read_b128 v[124:127], v162 offset:3072\n\t" \
    "ds_read_b128 v[128:131], v162 offset:4096\n\t" \
    "s_barrier\n\t" \
    "ds_read_b128 v[132:135], v160 offset:8192\n\t" \
    "ds_read_b128 v[140:143], v162 offset:10240\n\t" \
    "ds_read_b128 v[136:139], v161 offset:8192\n\t" \
    "ds_read_b128 v[144:147], v162 offset:11264\n\t" \
    "ds_read_b128 v[148:151], v162 offset:12288\n\t" \
    "ds_read_b128 v[152:155], v162 offset:13312\n\t" \
    "ds_read_b128 v[156:159], v162 offset:14336\n\t" \
    "s_waitcnt lgkmcnt(7)\n\t" \
    "s_barrier\n\t" \
    "v_mfma_f32_16x16x32_bf16 v[64:67], v[112:115], v[104:107], v[64:67]\n\t" \
    "ds_read_b128 v[166:169], v160 offset:16384\n\t" \
    "v_mfma_f32_16x16x32_bf16 v[84:87], v[112:115], v[108:111], v[84:87]\n\t" \
    "ds_read_b128 v[174:177], v162 offset:20480\n\t" \
    "v_mfma_f32_16x16x32_bf16 v[68:71], v[116:119], v[104:107], v[68:71]\n\t" \
    "ds_read_b128 v[170:173], v161 offset:16384\n\t" \
    "v_mfma_f32_16x16x32_bf16 v[88:91], v[116:119], v[108:111], v[88:91]\n\t" \
    "ds_read_b128 v[178:181], v162 offset:21504\n\t" \
    "v_mfma_f32_16x16x32_bf16 v[72:75], v[120:123], v[104:107], v[72:75]\n\t" \
    "ds_read_b128 v[182:185], v162 offset:22528\n\t" \
    "v_mfma_f32_16x16x32_bf16 v[92:95], v[120:123], v[108:111], v[92:95]\n\t" \
    "ds_read_b128 v[186:189], v162 offset:23552\n\t" \
    "v_mfma_f32_16x16x32_bf16 v[76:79], v[124:127], v[104:107], v[76:79]\n\t" \
    "ds_read_b128 v[190:193], v162 offset:24576\n\t" \
    "v_mfma_f32_16x16x32_bf16 v[96:99], v[124:127], v[108:111], v[96:99]\n\t" \
    "v_mfma_f32_16x16x32_bf16 v[80:83], v[128:131], v[104:107], v[80:83]\n\t" \
    "v_mfma_f32_16x16x32_bf16 v[100:103], v[128:131], v[108:111], v[100:103]\n\t" \
    "s_waitcnt lgkmcnt(7)\n\t" \
    "s_barrier\n\t" \
    "v_mfma_f32_16x16x32_bf16 v[64:67], v[140:143], v[132:135], v[64:67]\n\t" \
    "ds_read_b128 v[104:107], v160 offset:24576\n\t" \
    "v_mfma_f32_16x16x32_bf16 v[84:87], v[140:143], v[136:139], v[84:87]\n\t" \
    "ds_read_b128 v[112:115], v162 offset:30720\n\t" \
    "v_mfma_f32_16x16x32_bf16 v[68:71], v[144:147], v[132:135], v[68:71]\n\t" \
    "ds_read_b128 v[108:111], v161 offset:24576\n\t" \
    "v_mfma_f32_16x16x32_bf16 v[88:91], v[144:147], v[136:139], v[88:91]\n\t" \
    "ds_read_b128 v[116:119], v162 offset:31744\n\t" \
    "v_mfma_f32_16x16x32_bf16 v[72:75], v[148:151], v[132:135], v[72:75]\n\t" \
    "ds_read_b128 v[120:123], v162 offset:32768\n\t" \
    "v_mfma_f32_16x16x32_bf16 v[92:95], v[148:151], v[136:139], v[92:95]\n\t" \
    "ds_read_b128 v[124:127], v162 offset:33792\n\t" \
    "v_mfma_f32_16x16x32_bf16 v[76:79], v[152:155], v[132:135], v[76:79]\n\t" \
    "ds_read_b128 v[128:131], v162 offset:34816\n\t" \
    "v_mfma_f32_16x16x32_bf16 v[96:99], v[152:155], v[136:139], v[96:99]\n\t" \
    "v_mfma_f32_16x16x32_bf16 v[80:83], v[156:159], v[132:135], v[80:83]\n\t" \
    "v_mfma_f32_16x16x32_bf16 v[100:103], v[156:159], v[136:139], v[100:103]\n\t" \
    "s_waitcnt lgkmcnt(7)\n\t" \
    "s_barrier\n\t" \
    "v_mfma_f32_16x16x32_bf16 v[64:67], v[174:177], v[166:169], v[64:67]\n\t" \
    "ds_read_b128 v[132:135], v160 offset:32768\n\t" \
    "v_mfma_f32_16x16x32_bf16 v[84:87], v[174:177], v[170:173], v[84:87]\n\t" \
    "ds_read_b128 v[140:143], v162 offset:40960\n\t" \
    "v_mfma_f32_16x16x32_bf16 v[68:71], v[178:181], v[166:169], v[68:71]\n\t" \
    "ds_read_b128 v[136:139], v161 offset:32768\n\t" \
    "v_mfma_f32_16x16x32_bf16 v[88:91], v[178:181], v[170:173], v[88:91]\n\t" \
    "ds_read_b128 v[144:147], v162 offset:41984\n\t" \
    "v_mfma_f32_16x16x32_bf16 v[72:75], v[182:185], v[166:169], v[72:75]\n\t" \
    "ds_read_b128 v[148:151], v162 offset:43008\n\t" \
    "v_mfma_f32_16x16x32_bf16 v[92:95], v[182:185], v[170:173], v[92:95]\n\t" \
    "ds_read_b128 v[152:155], v162 offset:44032\n\t" \
    "v_mfma_f32_16x16x32_bf16 v[76:79], v[186:189], v[166:169], v[76:79]\n\t" \
    "ds_read_b128 v[156:159], v162 offset:45056\n\t" \
    "v_mfma_f32_16x16x32_bf16 v[96:99], v[186:189], v[170:173], v[96:99]\n\t" \
    "v_mfma_f32_16x16x32_bf16 v[80:83], v[190:193], v[166:169], v[80:83]\n\t" \
    "v_mfma_f32_16x16x32_bf16 v[100:103], v[190:193], v[170:173], v[100:103]\n\t" \
    "s_waitcnt lgkmcnt(7)\n\t" \
    "s_barrier\n\t" \
    "v_mfma_f32_16x16x32_bf16 v[64:67], v[112:115], v[104:107], v[64:67]\n\t" \
    "ds_read_b128 v[166:169], v160 offset:40960\n\t" \
    "v_mfma_f32_16x16x32_bf16 v[84:87], v[112:115], v[108:111], v[84:87]\n\t" \
    "ds_read_b128 v[174:177], v162 offset:51200\n\t" \
    "v_mfma_f32_16x16x32_bf16 v[68:71], v[116:119], v[104:107], v[68:71]\n\t" \
    "ds_read_b128 v[170:173], v161 offset:40960\n\t" \
    "v_mfma_f32_16x16x32_bf16 v[88:91], v[116:119], v[108:111], v[88:91]\n\t" \
    "ds_read_b128 v[178:181], v162 offset:52224\n\t" \
    "v_mfma_f32_16x16x32_bf16 v[72:75], v[120:123], v[104:107], v[72:75]\n\t" \
    "ds_read_b128 v[182:185], v162 offset:53248\n\t" \
    "v_mfma_f32_16x16x32_bf16 v[92:95], v[120:123], v[108:111], v[92:95]\n\t" \
    "ds_read_b128 v[186:189], v162 offset:54272\n\t" \
    "v_mfma_f32_16x16x32_bf16 v[76:79], v[124:127], v[104:107], v[76:79]\n\t" \
    "ds_read_b128 v[190:193], v162 offset:55296\n\t" \
    "v_mfma_f32_16x16x32_bf16 v[96:99], v[124:127], v[108:111], v[96:99]\n\t" \
    "v_mfma_f32_16x16x32_bf16 v[80:83], v[128:131], v[104:107], v[80:83]\n\t" \
    "v_mfma_f32_16x16x32_bf16 v[100:103], v[128:131], v[108:111], v[100:103]\n\t" \
    "s_waitcnt lgkmcnt(7)\n\t" \
    "s_barrier\n\t" \
    "v_mfma_f32_16x16x32_bf16 v[64:67], v[140:143], v[132:135], v[64:67]\n\t" \
    "ds_read_b128 v[104:107], v160 offset:0\n\t" \
    "v_mfma_f32_16x16x32_bf16 v[84:87], v[140:143], v[136:139], v[84:87]\n\t" \
    "ds_read_b128 v[112:115], v163 offset:0\n\t" \
    "v_mfma_f32_16x16x32_bf16 v[68:71], v[144:147], v[132:135], v[68:71]\n\t" \
    "ds_read_b128 v[108:111], v161 offset:0\n\t" \
    "v_mfma_f32_16x16x32_bf16 v[88:91], v[144:147], v[136:139], v[88:91]\n\t" \
    "ds_read_b128 v[116:119], v163 offset:1024\n\t" \
    "v_mfma_f32_16x16x32_bf16 v[72:75], v[148:151], v[132:135], v[72:75]\n\t" \
    "ds_read_b128 v[120:123], v163 offset:2048\n\t" \
    "v_mfma_f32_16x16x32_bf16 v[92:95], v[148:151], v[136:139], v[92:95]\n\t" \
    "ds_read_b128 v[124:127], v163 offset:3072\n\t" \
    "v_mfma_f32_16x16x32_bf16 v[76:79], v[152:155], v[132:135], v[76:79]\n\t" \
    "ds_read_b128 v[128:131], v163 offset:4096\n\t" \
    "v_mfma_f32_16x16x32_bf16 v[96:99], v[152:155], v[136:139], v[96:99]\n\t" \
    "v_mfma_f32_16x16x32_bf16 v[80:83], v[156:159], v[132:135], v[80:83]\n\t" \
    "v_mfma_f32_16x16x32_bf16 v[100:103], v[156:159], v[136:139], v[100:103]\n\t" \
    "s_waitcnt lgkmcnt(7)\n\t" \
    "s_barrier\n\t" \
    "v_mfma_f32_16x16x32_bf16 v[64:67], v[174:177], v[166:169], v[64:67]\n\t" \
    "ds_read_b128 v[132:135], v160 offset:8192\n\t" \
    "v_mfma_f32_16x16x32_bf16 v[84:87], v[174:177], v[170:173], v[84:87]\n\t" \
    "ds_read_b128 v[140:143], v163 offset:10240\n\t" \
    "v_mfma_f32_16x16x32_bf16 v[68:71], v[178:181], v[166:169], v[68:71]\n\t" \
    "ds_read_b128 v[136:139], v161 offset:8192\n\t" \
    "v_mfma_f32_16x16x32_bf16 v[88:91], v[178:181], v[170:173], v[88:91]\n\t" \
    "ds_read_b128 v[144:147], v163 offset:11264\n\t" \
    "v_mfma_f32_16x16x32_bf16 v[72:75], v[182:185], v[166:169], v[72:75]\n\t" \
    "ds_read_b128 v[148:151], v163 offset:12288\n\t" \
    "v_mfma_f32_16x16x32_bf16 v[92:95], v[182:185], v[170:173], v[92:95]\n\t" \
    "ds_read_b128 v[152:155], v163 offset:13312\n\t" \
    "v_mfma_f32_16x16x32_bf16 v[76:79], v[186:189], v[166:169], v[76:79]\n\t" \
    "ds_read_b128 v[156:159], v163 offset:14336\n\t" \
    "v_mfma_f32_16x16x32_bf16 v[96:99], v[186:189], v[170:173], v[96:99]\n\t" \
    "v_mfma_f32_16x16x32_bf16 v[80:83], v[190:193], v[166:169], v[80:83]\n\t" \
    "v_mfma_f32_16x16x32_bf16 v[100:103], v[190:193], v[170:173], v[100:103]\n\t" \
    "s_waitcnt lgkmcnt(7)\n\t" \
    "s_barrier\n\t" \
    "v_mfma_f32_16x16x32_bf16 v[64:67], v[112:115], v[104:107], v[64:67]\n\t" \
    "ds_read_b128 v[166:169], v160 offset:16384\n\t" \
    "v_mfma_f32_16x16x32_bf16 v[84:87], v[112:115], v[108:111], v[84:87]\n\t" \
    "ds_read_b128 v[174:177], v163 offset:20480\n\t" \
    "v_mfma_f32_16x16x32_bf16 v[68:71], v[116:119], v[104:107], v[68:71]\n\t" \
    "ds_read_b128 v[170:173], v161 offset:16384\n\t" \
    "v_mfma_f32_16x16x32_bf16 v[88:91], v[116:119], v[108:111], v[88:91]\n\t" \
    "ds_read_b128 v[178:181], v163 offset:21504\n\t" \
    "v_mfma_f32_16x16x32_bf16 v[72:75], v[120:123], v[104:107], v[72:75]\n\t" \
    "ds_read_b128 v[182:185], v163 offset:22528\n\t" \
    "v_mfma_f32_16x16x32_bf16 v[92:95], v[120:123], v[108:111], v[92:95]\n\t" \
    "ds_read_b128 v[186:189], v163 offset:23552\n\t" \
    "v_mfma_f32_16x16x32_bf16 v[76:79], v[124:127], v[104:107], v[76:79]\n\t" \
    "ds_read_b128 v[190:193], v163 offset:24576\n\t" \
    "v_mfma_f32_16x16x32_bf16 v[96:99], v[124:127], v[108:111], v[96:99]\n\t" \
    "v_mfma_f32_16x16x32_bf16 v[80:83], v[128:131], v[104:107], v[80:83]\n\t" \
    "v_mfma_f32_16x16x32_bf16 v[100:103], v[128:131], v[108:111], v[100:103]\n\t" \
    "s_waitcnt lgkmcnt(7)\n\t" \
    "s_barrier\n\t" \
    "v_mfma_f32_16x16x32_bf16 v[64:67], v[140:143], v[132:135], v[64:67]\n\t" \
    "ds_read_b128 v[104:107], v160 offset:24576\n\t" \
    "v_mfma_f32_16x16x32_bf16 v[84:87], v[140:143], v[136:139], v[84:87]\n\t" \
    "ds_read_b128 v[112:115], v163 offset:30720\n\t" \
    "v_mfma_f32_16x16x32_bf16 v[68:71], v[144:147], v[132:135], v[68:71]\n\t" \
    "ds_read_b128 v[108:111], v161 offset:24576\n\t" \
    "v_mfma_f32_16x16x32_bf16 v[88:91], v[144:147], v[136:139], v[88:91]\n\t" \
    "ds_read_b128 v[116:119], v163 offset:31744\n\t" \
    "v_mfma_f32_16x16x32_bf16 v[72:75], v[148:151], v[132:135], v[72:75]\n\t" \
    "ds_read_b128 v[120:123], v163 offset:32768\n\t" \
    "v_mfma_f32_16x16x32_bf16 v[92:95], v[148:151], v[136:139], v[92:95]\n\t" \
    "ds_read_b128 v[124:127], v163 offset:33792\n\t" \
    "v_mfma_f32_16x16x32_bf16 v[76:79], v[152:155], v[132:135], v[76:79]\n\t" \
    "ds_read_b128 v[128:131], v163 offset:34816\n\t" \
    "v_mfma_f32_16x16x32_bf16 v[96:99], v[152:155], v[136:139], v[96:99]\n\t" \
    "v_mfma_f32_16x16x32_bf16 v[80:83], v[156:159], v[132:135], v[80:83]\n\t" \
    "v_mfma_f32_16x16x32_bf16 v[100:103], v[156:159], v[136:139], v[100:103]\n\t" \
    "s_waitcnt lgkmcnt(7)\n\t" \
    "s_barrier\n\t" \
    "v_mfma_f32_16x16x32_bf16 v[64:67], v[174:177], v[166:169], v[64:67]\n\t" \
    "ds_read_b128 v[132:135], v160 offset:32768\n\t" \
    "v_mfma_f32_16x16x32_bf16 v[84:87], v[174:177], v[170:173], v[84:87]\n\t" \
    "ds_read_b128 v[140:143], v162 offset:0\n\t" \
    "v_mfma_f32_16x16x32_bf16 v[68:71], v[178:181], v[166:169], v[68:71]\n\t" \
    "ds_read_b128 v[136:139], v161 offset:32768\n\t" \
    "v_mfma_f32_16x16x32_bf16 v[88:91], v[178:181], v[170:173], v[88:91]\n\t" \
    "ds_read_b128 v[144:147], v162 offset:1024\n\t" \
    "v_mfma_f32_16x16x32_bf16 v[72:75], v[182:185], v[166:169], v[72:75]\n\t" \
    "ds_read_b128 v[148:151], v162 offset:2048\n\t" \
    "v_mfma_f32_16x16x32_bf16 v[92:95], v[182:185], v[170:173], v[92:95]\n\t" \
    "ds_read_b128 v[152:155], v162 offset:3072\n\t" \
    "v_mfma_f32_16x16x32_bf16 v[76:79], v[186:189], v[166:169], v[76:79]\n\t" \
    "ds_read_b128 v[156:159], v162 offset:4096\n\t" \
    "v_mfma_f32_16x16x32_bf16 v[96:99], v[186:189], v[170:173], v[96:99]\n\t" \
    "v_mfma_f32_16x16x32_bf16 v[80:83], v[190:193], v[166:169], v[80:83]\n\t" \
    "v_mfma_f32_16x16x32_bf16 v[100:103], v[190:193], v[170:173], v[100:103]\n\t" \
    "s_waitcnt lgkmcnt(7)\n\t" \
    "s_barrier\n\t" \
    "v_mfma_f32_16x16x32_bf16 v[64:67], v[112:115], v[104:107], v[64:67]\n\t" \
    "ds_read_b128 v[166:169], v160 offset:40960\n\t" \
    "v_mfma_f32_16x16x32_bf16 v[84:87], v[112:115], v[108:111], v[84:87]\n\t" \
    "ds_read_b128 v[174:177], v162 offset:10240\n\t" \
    "v_mfma_f32_16x16x32_bf16 v[68:71], v[116:119], v[104:107], v[68:71]\n\t" \
    "ds_read_b128 v[170:173], v161 offset:40960\n\t" \
    "v_mfma_f32_16x16x32_bf16 v[88:91], v[116:119], v[108:111], v[88:91]\n\t" \
    "ds_read_b128 v[178:181], v162 offset:11264\n\t" \
    "v_mfma_f32_16x16x32_bf16 v[72:75], v[120:123], v[104:107], v[72:75]\n\t" \
    "ds_read_b128 v[182:185], v162 offset:12288\n\t" \
    "v_mfma_f32_16x16x32_bf16 v[92:95], v[120:123], v[108:111], v[92:95]\n\t" \
    "ds_read_b128 v[186:189], v162 offset:13312\n\t" \
    "v_mfma_f32_16x16x32_bf16 v[76:79], v[124:127], v[104:107], v[76:79]\n\t" \
    "ds_read_b128 v[190:193], v162 offset:14336\n\t" \
    "v_mfma_f32_16x16x32_bf16 v[96:99], v[124:127], v[108:111], v[96:99]\n\t" \
    "v_mfma_f32_16x16x32_bf16 v[80:83], v[128:131], v[104:107], v[80:83]\n\t" \
    "v_mfma_f32_16x16x32_bf16 v[100:103], v[128:131], v[108:111], v[100:103]\n\t" \
    "s_waitcnt lgkmcnt(7)\n\t" \
    "s_barrier\n\t" \
    "v_mfma_f32_16x16x32_bf16 v[64:67], v[140:143], v[132:135], v[64:67]\n\t" \
    "ds_read_b128 v[104:107], v160 offset:0\n\t" \
    "v_mfma_f32_16x16x32_bf16 v[84:87], v[140:143], v[136:139], v[84:87]\n\t" \
    "ds_read_b128 v[112:115], v162 offset:20480\n\t" \
    "v_mfma_f32_16x16x32_bf16 v[68:71], v[144:147], v[132:135], v[68:71]\n\t" \
    "ds_read_b128 v[108:111], v161 offset:0\n\t" \
    "v_mfma_f32_16x16x32_bf16 v[88:91], v[144:147], v[136:139], v[88:91]\n\t" \
    "ds_read_b128 v[116:119], v162 offset:21504\n\t" \
    "v_mfma_f32_16x16x32_bf16 v[72:75], v[148:151], v[132:135], v[72:75]\n\t" \
    "ds_read_b128 v[120:123], v162 offset:22528\n\t" \
    "v_mfma_f32_16x16x32_bf16 v[92:95], v[148:151], v[136:139], v[92:95]\n\t" \
    "ds_read_b128 v[124:127], v162 offset:23552\n\t" \
    "v_mfma_f32_16x16x32_bf16 v[76:79], v[152:155], v[132:135], v[76:79]\n\t" \
    "ds_read_b128 v[128:131], v162 offset:24576\n\t" \
    "v_mfma_f32_16x16x32_bf16 v[96:99], v[152:155], v[136:139], v[96:99]\n\t" \
    "v_mfma_f32_16x16x32_bf16 v[80:83], v[156:159], v[132:135], v[80:83]\n\t" \
    "v_mfma_f32_16x16x32_bf16 v[100:103], v[156:159], v[136:139], v[100:103]\n\t" \
    "s_waitcnt lgkmcnt(7)\n\t" \
    "s_barrier\n\t" \
    "v_mfma_f32_16x16x32_bf16 v[64:67], v[174:177], v[166:169], v[64:67]\n\t" \
    "ds_read_b128 v[132:135], v160 offset:8192\n\t" \
    "v_mfma_f32_16x16x32_bf16 v[84:87], v[174:177], v[170:173], v[84:87]\n\t" \
    "ds_read_b128 v[140:143], v162 offset:30720\n\t" \
    "v_mfma_f32_16x16x32_bf16 v[68:71], v[178:181], v[166:169], v[68:71]\n\t" \
    "ds_read_b128 v[136:139], v161 offset:8192\n\t" \
    "v_mfma_f32_16x16x32_bf16 v[88:91], v[178:181], v[170:173], v[88:91]\n\t" \
    "ds_read_b128 v[144:147], v162 offset:31744\n\t" \
    "v_mfma_f32_16x16x32_bf16 v[72:75], v[182:185], v[166:169], v[72:75]\n\t" \
    "ds_read_b128 v[148:151], v162 offset:32768\n\t" \
    "v_mfma_f32_16x16x32_bf16 v[92:95], v[182:185], v[170:173], v[92:95]\n\t" \
    "ds_read_b128 v[152:155], v162 offset:33792\n\t" \
    "v_mfma_f32_16x16x32_bf16 v[76:79], v[186:189], v[166:169], v[76:79]\n\t" \
    "ds_read_b128 v[156:159], v162 offset:34816\n\t" \
    "v_mfma_f32_16x16x32_bf16 v[96:99], v[186:189], v[170:173], v[96:99]\n\t" \
    "v_mfma_f32_16x16x32_bf16 v[80:83], v[190:193], v[166:169], v[80:83]\n\t" \
    "v_mfma_f32_16x16x32_bf16 v[100:103], v[190:193], v[170:173], v[100:103]\n\t" \
    "s_waitcnt lgkmcnt(7)\n\t" \
    "s_barrier\n\t" \
    "v_mfma_f32_16x16x32_bf16 v[64:67], v[112:115], v[104:107], v[64:67]\n\t" \
    "ds_read_b128 v[166:169], v160 offset:16384\n\t" \
    "v_mfma_f32_16x16x32_bf16 v[84:87], v[112:115], v[108:111], v[84:87]\n\t" \
    "ds_read_b128 v[174:177], v162 offset:40960\n\t" \
    "v_mfma_f32_16x16x32_bf16 v[68:71], v[116:119], v[104:107], v[68:71]\n\t" \
    "ds_read_b128 v[170:173], v161 offset:16384\n\t" \
    "v_mfma_f32_16x16x32_bf16 v[88:91], v[116:119], v[108:111], v[88:91]\n\t" \
    "ds_read_b128 v[178:181], v162 offset:41984\n\t" \
    "v_mfma_f32_16x16x32_bf16 v[72:75], v[120:123], v[104:107], v[72:75]\n\t" \
    "ds_read_b128 v[182:185], v162 offset:43008\n\t" \
    "v_mfma_f32_16x16x32_bf16 v[92:95], v[120:123], v[108:111], v[92:95]\n\t" \
    "ds_read_b128 v[186:189], v162 offset:44032\n\t" \
    "v_mfma_f32_16x16x32_bf16 v[76:79], v[124:127], v[104:107], v[76:79]\n\t" \
    "ds_read_b128 v[190:193], v162 offset:45056\n\t" \
    "v_mfma_f32_16x16x32_bf16 v[96:99], v[124:127], v[108:111], v[96:99]\n\t" \
    "v_mfma_f32_16x16x32_bf16 v[80:83], v[128:131], v[104:107], v[80:83]\n\t" \
    "v_mfma_f32_16x16x32_bf16 v[100:103], v[128:131], v[108:111], v[100:103]\n\t" \
    "s_waitcnt lgkmcnt(7)\n\t" \
    "s_barrier\n\t" \
    "v_mfma_f32_16x16x32_bf16 v[64:67], v[140:143], v[132:135], v[64:67]\n\t" \
    "ds_read_b128 v[104:107], v160 offset:24576\n\t" \
    "v_mfma_f32_16x16x32_bf16 v[84:87], v[140:143], v[136:139], v[84:87]\n\t" \
    "ds_read_b128 v[112:115], v162 offset:51200\n\t" \
    "v_mfma_f32_16x16x32_bf16 v[68:71], v[144:147], v[132:135], v[68:71]\n\t" \
    "ds_read_b128 v[108:111], v161 offset:24576\n\t" \
    "v_mfma_f32_16x16x32_bf16 v[88:91], v[144:147], v[136:139], v[88:91]\n\t" \
    "ds_read_b128 v[116:119], v162 offset:52224\n\t" \
    "v_mfma_f32_16x16x32_bf16 v[72:75], v[148:151], v[132:135], v[72:75]\n\t" \
    "ds_read_b128 v[120:123], v162 offset:53248\n\t" \
    "v_mfma_f32_16x16x32_bf16 v[92:95], v[148:151], v[136:139], v[92:95]\n\t" \
    "ds_read_b128 v[124:127], v162 offset:54272\n\t" \
    "v_mfma_f32_16x16x32_bf16 v[76:79], v[152:155], v[132:135], v[76:79]\n\t" \
    "ds_read_b128 v[128:131], v162 offset:55296\n\t" \
    "v_mfma_f32_16x16x32_bf16 v[96:99], v[152:155], v[136:139], v[96:99]\n\t" \
    "v_mfma_f32_16x16x32_bf16 v[80:83], v[156:159], v[132:135], v[80:83]\n\t" \
    "v_mfma_f32_16x16x32_bf16 v[100:103], v[156:159], v[136:139], v[100:103]\n\t" \
    "s_waitcnt lgkmcnt(7)\n\t" \
    "s_barrier\n\t" \
    "v_mfma_f32_16x16x32_bf16 v[64:67], v[174:177], v[166:169], v[64:67]\n\t" \
    "ds_read_b128 v[132:135], v160 offset:32768\n\t" \
    "v_mfma_f32_16x16x32_bf16 v[84:87], v[174:177], v[170:173], v[84:87]\n\t" \
    "ds_read_b128 v[140:143], v163 offset:0\n\t" \
    "v_mfma_f32_16x16x32_bf16 v[68:71], v[178:181], v[166:169], v[68:71]\n\t" \
    "ds_read_b128 v[136:139], v161 offset:32768\n\t" \
    "v_mfma_f32_16x16x32_bf16 v[88:91], v[178:181], v[170:173], v[88:91]\n\t" \
    "ds_read_b128 v[144:147], v163 offset:1024\n\t" \
    "v_mfma_f32_16x16x32_bf16 v[72:75], v[182:185], v[166:169], v[72:75]\n\t" \
    "ds_read_b128 v[148:151], v163 offset:2048\n\t" \
    "v_mfma_f32_16x16x32_bf16 v[92:95], v[182:185], v[170:173], v[92:95]\n\t" \
    "ds_read_b128 v[152:155], v163 offset:3072\n\t" \
    "v_mfma_f32_16x16x32_bf16 v[76:79], v[186:189], v[166:169], v[76:79]\n\t" \
    "ds_read_b128 v[156:159], v163 offset:4096\n\t" \
    "v_mfma_f32_16x16x32_bf16 v[96:99], v[186:189], v[170:173], v[96:99]\n\t" \
    "v_mfma_f32_16x16x32_bf16 v[80:83], v[190:193], v[166:169], v[80:83]\n\t" \
    "v_mfma_f32_16x16x32_bf16 v[100:103], v[190:193], v[170:173], v[100:103]\n\t" \
    "s_waitcnt lgkmcnt(7)\n\t" \
    "s_barrier\n\t" \
    "v_mfma_f32_16x16x32_bf16 v[64:67], v[112:115], v[104:107], v[64:67]\n\t" \
    "ds_read_b128 v[166:169], v160 offset:40960\n\t" \
    "v_mfma_f32_16x16x32_bf16 v[84:87], v[112:115], v[108:111], v[84:87]\n\t" \
    "ds_read_b128 v[174:177], v163 offset:10240\n\t" \
    "v_mfma_f32_16x16x32_bf16 v[68:71], v[116:119], v[104:107], v[68:71]\n\t" \
    "ds_read_b128 v[170:173], v161 offset:40960\n\t" \
    "v_mfma_f32_16x16x32_bf16 v[88:91], v[116:119], v[108:111], v[88:91]\n\t" \
    "ds_read_b128 v[178:181], v163 offset:11264\n\t" \
    "v_mfma_f32_16x16x32_bf16 v[72:75], v[120:123], v[104:107], v[72:75]\n\t" \
    "ds_read_b128 v[182:185], v163 offset:12288\n\t" \
    "v_mfma_f32_16x16x32_bf16 v[92:95], v[120:123], v[108:111], v[92:95]\n\t" \
    "ds_read_b128 v[186:189], v163 offset:13312\n\t" \
    "v_mfma_f32_16x16x32_bf16 v[76:79], v[124:127], v[104:107], v[76:79]\n\t" \
    "ds_read_b128 v[190:193], v163 offset:14336\n\t" \
    "v_mfma_f32_16x16x32_bf16 v[96:99], v[124:127], v[108:111], v[96:99]\n\t" \
    "v_mfma_f32_16x16x32_bf16 v[80:83], v[128:131], v[104:107], v[80:83]\n\t" \
    "v_mfma_f32_16x16x32_bf16 v[100:103], v[128:131], v[108:111], v[100:103]\n\t" \
    "s_waitcnt lgkmcnt(7)\n\t" \
    "s_barrier\n\t" \
    "v_mfma_f32_16x16x32_bf16 v[64:67], v[140:143], v[132:135], v[64:67]\n\t" \
    "ds_read_b128 v[104:107], v160 offset:0\n\t" \
    "v_mfma_f32_16x16x32_bf16 v[84:87], v[140:143], v[136:139], v[84:87]\n\t" \
    "ds_read_b128 v[112:115], v163 offset:20480\n\t" \
    "v_mfma_f32_16x16x32_bf16 v[68:71], v[144:147], v[132:135], v[68:71]\n\t" \
    "ds_read_b128 v[108:111], v161 offset:0\n\t" \
    "v_mfma_f32_16x16x32_bf16 v[88:91], v[144:147], v[136:139], v[88:91]\n\t" \
    "ds_read_b128 v[116:119], v163 offset:21504\n\t" \
    "v_mfma_f32_16x16x32_bf16 v[72:75], v[148:151], v[132:135], v[72:75]\n\t" \
    "ds_read_b128 v[120:123], v163 offset:22528\n\t" \
    "v_mfma_f32_16x16x32_bf16 v[92:95], v[148:151], v[136:139], v[92:95]\n\t" \
    "ds_read_b128 v[124:127], v163 offset:23552\n\t" \
    "v_mfma_f32_16x16x32_bf16 v[76:79], v[152:155], v[132:135], v[76:79]\n\t" \
    "ds_read_b128 v[128:131], v163 offset:24576\n\t" \
    "v_mfma_f32_16x16x32_bf16 v[96:99], v[152:155], v[136:139], v[96:99]\n\t" \
    "v_mfma_f32_16x16x32_bf16 v[80:83], v[156:159], v[132:135], v[80:83]\n\t" \
    "v_mfma_f32_16x16x32_bf16 v[100:103], v[156:159], v[136:139], v[100:103]\n\t" \
    "s_waitcnt lgkmcnt(7)\n\t" \
    "s_barrier\n\t" \
    "v_mfma_f32_16x16x32_bf16 v[64:67], v[174:177], v[166:169], v[64:67]\n\t" \
    "ds_read_b128 v[132:135], v160 offset:8192\n\t" \
    "v_mfma_f32_16x16x32_bf16 v[84:87], v[174:177], v[170:173], v[84:87]\n\t" \
    "ds_read_b128 v[140:143], v163 offset:30720\n\t" \
    "v_mfma_f32_16x16x32_bf16 v[68:71], v[178:181], v[166:169], v[68:71]\n\t" \
    "ds_read_b128 v[136:139], v161 offset:8192\n\t" \
    "v_mfma_f32_16x16x32_bf16 v[88:91], v[178:181], v[170:173], v[88:91]\n\t" \
    "ds_read_b128 v[144:147], v163 offset:31744\n\t" \
    "v_mfma_f32_16x16x32_bf16 v[72:75], v[182:185], v[166:169], v[72:75]\n\t" \
    "ds_read_b128 v[148:151], v163 offset:32768\n\t" \
    "v_mfma_f32_16x16x32_bf16 v[92:95], v[182:185], v[170:173], v[92:95]\n\t" \
    "ds_read_b128 v[152:155], v163 offset:33792\n\t" \
    "v_mfma_f32_16x16x32_bf16 v[76:79], v[186:189], v[166:169], v[76:79]\n\t" \
    "ds_read_b128 v[156:159], v163 offset:34816\n\t" \
    "v_mfma_f32_16x16x32_bf16 v[96:99], v[186:189], v[170:173], v[96:99]\n\t" \
    "v_mfma_f32_16x16x32_bf16 v[80:83], v[190:193], v[166:169], v[80:83]\n\t" \
    "v_mfma_f32_16x16x32_bf16 v[100:103], v[190:193], v[170:173], v[100:103]\n\t" \
    "s_waitcnt lgkmcnt(7)\n\t" \
    "s_barrier\n\t" \
    "v_mfma_f32_16x16x32_bf16 v[64:67], v[112:115], v[104:107], v[64:67]\n\t" \
    "ds_read_b128 v[166:169], v160 offset:16384\n\t" \
    "v_mfma_f32_16x16x32_bf16 v[84:87], v[112:115], v[108:111], v[84:87]\n\t" \
    "ds_read_b128 v[174:177], v162 offset:0\n\t" \
    "v_mfma_f32_16x16x32_bf16 v[68:71], v[116:119], v[104:107], v[68:71]\n\t" \
    "ds_read_b128 v[170:173], v161 offset:16384\n\t" \
    "v_mfma_f32_16x16x32_bf16 v[88:91], v[116:119], v[108:111], v[88:91]\n\t" \
    "ds_read_b128 v[178:181], v162 offset:1024\n\t" \
    "v_mfma_f32_16x16x32_bf16 v[72:75], v[120:123], v[104:107], v[72:75]\n\t" \
    "ds_read_b128 v[182:185], v162 offset:2048\n\t" \
    "v_mfma_f32_16x16x32_bf16 v[92:95], v[120:123], v[108:111], v[92:95]\n\t" \
    "ds_read_b128 v[186:189], v162 offset:3072\n\t" \
    "v_mfma_f32_16x16x32_bf16 v[76:79], v[124:127], v[104:107], v[76:79]\n\t" \
    "ds_read_b128 v[190:193], v162 offset:4096\n\t" \
    "v_mfma_f32_16x16x32_bf16 v[96:99], v[124:127], v[108:111], v[96:99]\n\t" \
    "v_mfma_f32_16x16x32_bf16 v[80:83], v[128:131], v[104:107], v[80:83]\n\t" \
    "v_mfma_f32_16x16x32_bf16 v[100:103], v[128:131], v[108:111], v[100:103]\n\t" \
    "s_waitcnt lgkmcnt(7)\n\t" \
    "s_barrier\n\t" \
    "v_mfma_f32_16x16x32_bf16 v[64:67], v[140:143], v[132:135], v[64:67]\n\t" \
    "ds_read_b128 v[104:107], v160 offset:24576\n\t" \
    "v_mfma_f32_16x16x32_bf16 v[84:87], v[140:143], v[136:139], v[84:87]\n\t" \
    "ds_read_b128 v[112:115], v162 offset:10240\n\t" \
    "v_mfma_f32_16x16x32_bf16 v[68:71], v[144:147], v[132:135], v[68:71]\n\t" \
    "ds_read_b128 v[108:111], v161 offset:24576\n\t" \
    "v_mfma_f32_16x16x32_bf16 v[88:91], v[144:147], v[136:139], v[88:91]\n\t" \
    "ds_read_b128 v[116:119], v162 offset:11264\n\t" \
    "v_mfma_f32_16x16x32_bf16 v[72:75], v[148:151], v[132:135], v[72:75]\n\t" \
    "ds_read_b128 v[120:123], v162 offset:12288\n\t" \
    "v_mfma_f32_16x16x32_bf16 v[92:95], v[148:151], v[136:139], v[92:95]\n\t" \
    "ds_read_b128 v[124:127], v162 offset:13312\n\t" \
    "v_mfma_f32_16x16x32_bf16 v[76:79], v[152:155], v[132:135], v[76:79]\n\t" \
    "ds_read_b128 v[128:131], v162 offset:14336\n\t" \
    "v_mfma_f32_16x16x32_bf16 v[96:99], v[152:155], v[136:139], v[96:99]\n\t" \
    "v_mfma_f32_16x16x32_bf16 v[80:83], v[156:159], v[132:135], v[80:83]\n\t" \
    "v_mfma_f32_16x16x32_bf16 v[100:103], v[156:159], v[136:139], v[100:103]\n\t" \
    "s_waitcnt lgkmcnt(7)\n\t" \
    "s_barrier\n\t" \
    "v_mfma_f32_16x16x32_bf16 v[64:67], v[174:177], v[166:169], v[64:67]\n\t" \
    "ds_read_b128 v[132:135], v160 offset:32768\n\t" \
    "v_mfma_f32_16x16x32_bf16 v[84:87], v[174:177], v[170:173], v[84:87]\n\t" \
    "ds_read_b128 v[140:143], v162 offset:20480\n\t" \
    "v_mfma_f32_16x16x32_bf16 v[68:71], v[178:181], v[166:169], v[68:71]\n\t" \
    "ds_read_b128 v[136:139], v161 offset:32768\n\t" \
    "v_mfma_f32_16x16x32_bf16 v[88:91], v[178:181], v[170:173], v[88:91]\n\t" \
    "ds_read_b128 v[144:147], v162 offset:21504\n\t" \
    "v_mfma_f32_16x16x32_bf16 v[72:75], v[182:185], v[166:169], v[72:75]\n\t" \
    "ds_read_b128 v[148:151], v162 offset:22528\n\t" \
    "v_mfma_f32_16x16x32_bf16 v[92:95], v[182:185], v[170:173], v[92:95]\n\t" \
    "ds_read_b128 v[152:155], v162 offset:23552\n\t" \
    "v_mfma_f32_16x16x32_bf16 v[76:79], v[186:189], v[166:169], v[76:79]\n\t" \
    "ds_read_b128 v[156:159], v162 offset:24576\n\t" \
    "v_mfma_f32_16x16x32_bf16 v[96:99], v[186:189], v[170:173], v[96:99]\n\t" \
    "v_mfma_f32_16x16x32_bf16 v[80:83], v[190:193], v[166:169], v[80:83]\n\t" \
    "v_mfma_f32_16x16x32_bf16 v[100:103], v[190:193], v[170:173], v[100:103]\n\t" \
    "s_waitcnt lgkmcnt(7)\n\t" \
    "s_barrier\n\t" \
    "v_mfma_f32_16x16x32_bf16 v[64:67], v[112:115], v[104:107], v[64:67]\n\t" \
    "ds_read_b128 v[166:169], v160 offset:40960\n\t" \
    "v_mfma_f32_16x16x32_bf16 v[84:87], v[112:115], v[108:111], v[84:87]\n\t" \
    "ds_read_b128 v[174:177], v162 offset:30720\n\t" \
    "v_mfma_f32_16x16x32_bf16 v[68:71], v[116:119], v[104:107], v[68:71]\n\t" \
    "ds_read_b128 v[170:173], v161 offset:40960\n\t" \
    "v_mfma_f32_16x16x32_bf16 v[88:91], v[116:119], v[108:111], v[88:91]\n\t" \
    "ds_read_b128 v[178:181], v162 offset:31744\n\t" \
    "v_mfma_f32_16x16x32_bf16 v[72:75], v[120:123], v[104:107], v[72:75]\n\t" \
    "ds_read_b128 v[182:185], v162 offset:32768\n\t" \
    "v_mfma_f32_16x16x32_bf16 v[92:95], v[120:123], v[108:111], v[92:95]\n\t" \
    "ds_read_b128 v[186:189], v162 offset:33792\n\t" \
    "v_mfma_f32_16x16x32_bf16 v[76:79], v[124:127], v[104:107], v[76:79]\n\t" \
    "ds_read_b128 v[190:193], v162 offset:34816\n\t" \
    "v_mfma_f32_16x16x32_bf16 v[96:99], v[124:127], v[108:111], v[96:99]\n\t" \
    "v_mfma_f32_16x16x32_bf16 v[80:83], v[128:131], v[104:107], v[80:83]\n\t" \
    "v_mfma_f32_16x16x32_bf16 v[100:103], v[128:131], v[108:111], v[100:103]\n\t" \
    "s_waitcnt lgkmcnt(7)\n\t" \
    "s_barrier\n\t" \
    "v_mfma_f32_16x16x32_bf16 v[64:67], v[140:143], v[132:135], v[64:67]\n\t" \
    "ds_read_b128 v[104:107], v160 offset:0\n\t" \
    "v_mfma_f32_16x16x32_bf16 v[84:87], v[140:143], v[136:139], v[84:87]\n\t" \
    "ds_read_b128 v[112:115], v162 offset:40960\n\t" \
    "v_mfma_f32_16x16x32_bf16 v[68:71], v[144:147], v[132:135], v[68:71]\n\t" \
    "ds_read_b128 v[108:111], v161 offset:0\n\t" \
    "v_mfma_f32_16x16x32_bf16 v[88:91], v[144:147], v[136:139], v[88:91]\n\t" \
    "ds_read_b128 v[116:119], v162 offset:41984\n\t" \
    "v_mfma_f32_16x16x32_bf16 v[72:75], v[148:151], v[132:135], v[72:75]\n\t" \
    "ds_read_b128 v[120:123], v162 offset:43008\n\t" \
    "v_mfma_f32_16x16x32_bf16 v[92:95], v[148:151], v[136:139], v[92:95]\n\t" \
    "ds_read_b128 v[124:127], v162 offset:44032\n\t" \
    "v_mfma_f32_16x16x32_bf16 v[76:79], v[152:155], v[132:135], v[76:79]\n\t" \
    "ds_read_b128 v[128:131], v162 offset:45056\n\t" \
    "v_mfma_f32_16x16x32_bf16 v[96:99], v[152:155], v[136:139], v[96:99]\n\t" \
    "v_mfma_f32_16x16x32_bf16 v[80:83], v[156:159], v[132:135], v[80:83]\n\t" \
    "v_mfma_f32_16x16x32_bf16 v[100:103], v[156:159], v[136:139], v[100:103]\n\t" \
    "s_waitcnt lgkmcnt(7)\n\t" \
    "s_barrier\n\t" \
    "v_mfma_f32_16x16x32_bf16 v[64:67], v[174:177], v[166:169], v[64:67]\n\t" \
    "ds_read_b128 v[132:135], v160 offset:8192\n\t" \
    "v_mfma_f32_16x16x32_bf16 v[84:87], v[174:177], v[170:173], v[84:87]\n\t" \
    "ds_read_b128 v[140:143], v162 offset:51200\n\t" \
    "v_mfma_f32_16x16x32_bf16 v[68:71], v[178:181], v[166:169], v[68:71]\n\t" \
    "ds_read_b128 v[136:139], v161 offset:8192\n\t" \
    "v_mfma_f32_16x16x32_bf16 v[88:91], v[178:181], v[170:173], v[88:91]\n\t" \
    "ds_read_b128 v[144:147], v162 offset:52224\n\t" \
    "v_mfma_f32_16x16x32_bf16 v[72:75], v[182:185], v[166:169], v[72:75]\n\t" \
    "ds_read_b128 v[148:151], v162 offset:53248\n\t" \
    "v_mfma_f32_16x16x32_bf16 v[92:95], v[182:185], v[170:173], v[92:95]\n\t" \
    "ds_read_b128 v[152:155], v162 offset:54272\n\t" \
    "v_mfma_f32_16x16x32_bf16 v[76:79], v[186:189], v[166:169], v[76:79]\n\t" \
    "ds_read_b128 v[156:159], v162 offset:55296\n\t" \
    "v_mfma_f32_16x16x32_bf16 v[96:99], v[186:189], v[170:173], v[96:99]\n\t" \
    "v_mfma_f32_16x16x32_bf16 v[80:83], v[190:193], v[166:169], v[80:83]\n\t" \
    "v_mfma_f32_16x16x32_bf16 v[100:103], v[190:193], v[170:173], v[100:103]\n\t" \
    "s_waitcnt lgkmcnt(7)\n\t" \
    "s_barrier\n\t" \
    "v_mfma_f32_16x16x32_bf16 v[64:67], v[112:115], v[104:107], v[64:67]\n\t" \
    "ds_read_b128 v[166:169], v160 offset:16384\n\t" \
    "v_mfma_f32_16x16x32_bf16 v[84:87], v[112:115], v[108:111], v[84:87]\n\t" \
    "ds_read_b128 v[174:177], v163 offset:0\n\t" \
    "v_mfma_f32_16x16x32_bf16 v[68:71], v[116:119], v[104:107], v[68:71]\n\t" \
    "ds_read_b128 v[170:173], v161 offset:16384\n\t" \
    "v_mfma_f32_16x16x32_bf16 v[88:91], v[116:119], v[108:111], v[88:91]\n\t" \
    "ds_read_b128 v[178:181], v163 offset:1024\n\t" \
    "v_mfma_f32_16x16x32_bf16 v[72:75], v[120:123], v[104:107], v[72:75]\n\t" \
    "ds_read_b128 v[182:185], v163 offset:2048\n\t" \
    "v_mfma_f32_16x16x32_bf16 v[92:95], v[120:123], v[108:111], v[92:95]\n\t" \
    "ds_read_b128 v[186:189], v163 offset:3072\n\t" \
    "v_mfma_f32_16x16x32_bf16 v[76:79], v[124:127], v[104:107], v[76:79]\n\t" \
    "ds_read_b128 v[190:193], v163 offset:4096\n\t" \
    "v_mfma_f32_16x16x32_bf16 v[96:99], v[124:127], v[108:111], v[96:99]\n\t" \
    "v_mfma_f32_16x16x32_bf16 v[80:83], v[128:131], v[104:107], v[80:83]\n\t" \
    "v_mfma_f32_16x16x32_bf16 v[100:103], v[128:131], v[108:111], v[100:103]\n\t" \
    "s_waitcnt lgkmcnt(7)\n\t" \
    "v_mfma_f32_16x16x32_bf16 v[64:67], v[140:143], v[132:135], v[64:67]\n\t" \
    "v_mfma_f32_16x16x32_bf16 v[84:87], v[140:143], v[136:139], v[84:87]\n\t" \
    "v_mfma_f32_16x16x32_bf16 v[68:71], v[144:147], v[132:135], v[68:71]\n\t" \
    "v_mfma_f32_16x16x32_bf16 v[88:91], v[144:147], v[136:139], v[88:91]\n\t" \
    "v_mfma_f32_16x16x32_bf16 v[72:75], v[148:151], v[132:135], v[72:75]\n\t" \
    "v_mfma_f32_16x16x32_bf16 v[92:95], v[148:151], v[136:139], v[92:95]\n\t" \
    "v_mfma_f32_16x16x32_bf16 v[76:79], v[152:155], v[132:135], v[76:79]\n\t" \
    "v_mfma_f32_16x16x32_bf16 v[96:99], v[152:155], v[136:139], v[96:99]\n\t" \
    "v_mfma_f32_16x16x32_bf16 v[80:83], v[156:159], v[132:135], v[80:83]\n\t" \
    "v_mfma_f32_16x16x32_bf16 v[100:103], v[156:159], v[136:139], v[100:103]\n\t" \
    "s_waitcnt lgkmcnt(0)\n\t" \
    "v_mfma_f32_16x16x32_bf16 v[64:67], v[174:177], v[166:169], v[64:67]\n\t" \
    "v_mfma_f32_16x16x32_bf16 v[84:87], v[174:177], v[170:173], v[84:87]\n\t" \
    "v_mfma_f32_16x16x32_bf16 v[68:71], v[178:181], v[166:169], v[68:71]\n\t" \
    "v_mfma_f32_16x16x32_bf16 v[88:91], v[178:181], v[170:173], v[88:91]\n\t" \
    "v_mfma_f32_16x16x32_bf16 v[72:75], v[182:185], v[166:169], v[72:75]\n\t" \
    "v_mfma_f32_16x16x32_bf16 v[92:95], v[182:185], v[170:173], v[92:95]\n\t" \
    "v_mfma_f32_16x16x32_bf16 v[76:79], v[186:189], v[166:169], v[76:79]\n\t" \
    "v_mfma_f32_16x16x32_bf16 v[96:99], v[186:189], v[170:173], v[96:99]\n\t" \
    "v_mfma_f32_16x16x32_bf16 v[80:83], v[190:193], v[166:169], v[80:83]\n\t" \
    "v_mfma_f32_16x16x32_bf16 v[100:103], v[190:193], v[170:173], v[100:103]\n\t" \
    "s_nop 15\n\t" \
    ""

struct T2Tile {
    int rb, cb;
    __device__ __forceinline__ T2Tile() {
        const int xcd = blockIdx.x & 7, j = blockIdx.x >> 3;
        rb = (xcd >> 1) * 8 + (j & 7); cb = (xcd & 1) * 4 + (j >> 3);
    }
};
template <int MODE> struct T2Loader {
    static constexpr int K = MODE == 0 ? D_ : F_, NST = K / 64, ATILE = K * 64;
    rsrc_t r; unsigned v[4], s0, l0; bool isB;
    __device__ __forceinline__ T2Loader(const Args& a, unsigned lds0, int w, int lane) {
        const T2Tile tile;
        isB = w >= 6;
        const int i = w & 1;
        if (!isB) {
            r = MODE == 0 ? mk_rsrc(a.ws + WS_O, S_ * D_ * 2) : mk_rsrc(a.ws + WS_H1, S_ * F_ * 2);
#pragma unroll
            for (int p = 0; p < 4; ++p) v[p] = (lane >> 5) * ATILE + (4 * i + p) * 512 + (lane & 31) * 16;
            s0 = tile.rb * 2 * ATILE;
            l0 = lds0 + T2_A + i * 4096;
        } else {
            r = MODE == 0 ? mk_rsrc(a.ws + WS_WT0, 8 * 10 * 10240) : mk_rsrc(a.ws + WS_WT2, 8 * 27 * 10240);
            v[0] = lane * 16; v[1] = v[2] = v[3] = 0;
            s0 = tile.cb * NST * 10240 + i * 5120;
            l0 = lds0 + T2_B + i * 5120;
        }
    }
    __device__ __forceinline__ void issueA(int c) const {
#pragma unroll
        for (int p = 0; p < 4; ++p) dma16<true>(l0 + (c % T2_NA) * 8192 + p * 1024, v[p], r, s0 + c * 4096);
    }
    __device__ __forceinline__ void issueB(int c) const {
#pragma unroll
        for (int p = 0; p < 5; ++p) dma16<true>(l0 + (c % T2_NB) * 10240 + p * 1024, v[0], r, s0 + c * 10240 + p * 1024);
    }
    static __device__ __forceinline__ unsigned fw_off(int lane) { return MODE == 0 ? WS_FW + (lane & 15) * FL : WS_FW + (16 + (lane & 31)) * FL; }
    template <class H> __device__ __forceinline__ void run(H&& hook) const {
        if (!isB) {
#pragma unroll
            for (int c = 0; c < T2_NA; ++c) issueA(c);
            ring_steps<NST, T2_NA, 4, 1, 0>([&](int c) { issueA(c); }, hook);
        } else {
            WAIT_VM(0);
            ring_steps<NST, T2_NB, 5, 1, 0>([&](int c) { issueB(c); }, hook);
        }
    }
};

template <int MODE, int C0, int C1>
__device__ __forceinline__ void wt_preload(const Args& a, unsigned lds0, int lw, int lane, rsrc_t rws, Tag T, u32x2 seen) {
    constexpr int NST = MODE == 0 ? 10 : 27, NP = (C1 - C0) * 10;
    const T2Tile tile;
    if (!__all(seen[0] == T.lo && seen[1] == T.hi)) wait_flags(rws, T2Loader<MODE>::fw_off(lane), T);
    const rsrc_t r = MODE == 0 ? mk_rsrc(a.ws + WS_WT0, 8 * 10 * 10240) : mk_rsrc(a.ws + WS_WT2, 8 * 27 * 10240);
#pragma unroll
    for (int i = 0; i < (NP + 3) / 4; ++i) {
        const int q = 10 * C0 + 4 * i + lw;
        if (4 * i + 3 < NP || lw < NP - 4 * i) dma16<true>(lds0 + T2_B + q * 1024, lane * 16, r, tile.cb * NST * 10240 + q * 1024);
    }
}

template <int MODE>
__device__ __forceinline__ void t2_consume(unsigned lds0, int w, int lane, f32x4 (&acc0)[5], f32x4 (&acc1)[5]) {
    const int rh = w >> 1, kg = w & 1, r16 = lane & 15, kq = lane >> 4;
    unsigned aA[2];
#pragma unroll
    for (int t = 0; t < 2; ++t)
        aA[t] = lds0 + T2_A + (4 * kg + kq) * 1024 + (32 * rh + 16 * (t ^ kg) + r16) * 16;
    const unsigned aB = lds0 + T2_B + kg * 5120 + lane * 16;
    f32x4 c0 = {}, c1 = {}, c2 = {}, c3 = {}, c4 = {}, c5 = {}, c6 = {}, c7 = {}, c8 = {}, c9 = {};
#define T2_OPERANDS \
        : "+{v[64:67]}"(c0), "+{v[68:71]}"(c1), "+{v[72:75]}"(c2), "+{v[76:79]}"(c3), "+{v[80:83]}"(c4), \
          "+{v[84:87]}"(c5), "+{v[88:91]}"(c6), "+{v[92:95]}"(c7), "+{v[96:99]}"(c8), "+{v[100:103]}"(c9) \
        : "{v160}"(aA[0]), "{v161}"(aA[1]), "{v162}"(aB), "{v163}"(aB + 6 * 10240) \
        : "memory", "v104", "v105", "v106", "v107", "v108", "v109", "v110", "v111", "v112", "v113", "v114", "v115", "v116", "v117", "v118", "v119", \
          "v120", "v121", "v122", "v123", "v124", "v125", "v126", "v127", "v128", "v129", "v130", "v131", "v132", "v133", "v134", "v135", \
          "v136", "v137", "v138", "v139", "v140", "v141", "v142", "v143", "v144", "v145", "v146", "v147", "v148", "v149", "v150", "v151", \
          "v152", "v153", "v154", "v155", "v156", "v157", "v158", "v159", "v166", "v167", "v168", "v169", "v170", "v171", "v172", "v173", \
          "v174", "v175", "v176", "v177", "v178", "v179", "v180", "v181", "v182", "v183", "v184", "v185", "v186", "v187", "v188", "v189", \
          "v190", "v191", "v192", "v193"
    if (MODE == 0) asm volatile(T2_LOOP_ASM_10 T2_OPERANDS);
    else           asm volatile(T2_LOOP_ASM_27 T2_OPERANDS);
#undef T2_OPERANDS
    acc0[0] = c0; acc0[1] = c1; acc0[2] = c2; acc0[3] = c3; acc0[4] = c4;
    acc1[0] = c5; acc1[1] = c6; acc1[2] = c7; acc1[3] = c8; acc1[4] = c9;
}

__device__ __forceinline__ void retile_issue(const void* W, int K, int cb, int c, unsigned lw, int lane) {
    const rsrc_t rW = mk_rsrc(W, K * D_ * 2);
#pragma unroll
    for (int p = 0; p < 10; ++p) {
        const int sl = 64 * p + lane, lam = sl / 10, ch = sl - lam * 10;
        const int k = (lam & ~12) | ((lam & 4) << 1) | ((lam & 8) >> 1);
        dma16<false>(lw + p * 1024, k * (D_ * 2) + ch * 16, rW, cb * 160 + c * 64 * (D_ * 2));
    }
}
__device__ __forceinline__ void retile_finish(rsrc_t rws, unsigned dst, unsigned lw, int lane) {
    WAIT_VM(0);
    const int r16 = lane & 15, kq = lane >> 4, q = r16 >> 2, pp = r16 & 3;
    unsigned ad[2];
#pragma unroll
    for (int jj = 0; jj < 2; ++jj) {
        const int k = 8 * kq + 4 * jj + q;
        ad[jj] = lw + ((k & ~12) | ((k & 4) << 1) | ((k & 8) >> 1)) * 160 + pp * 8;
    }
#pragma unroll
    for (int ks = 0; ks < 2; ++ks)
#pragma unroll
        for (int n = 0; n < 5; ++n) {
            const s16x4 lo = __builtin_amdgcn_ds_read_tr16_b64_v4i16((LAS s16x4*)(ad[0] + ks * 5120 + 32 * n));
            const s16x4 hi = __builtin_amdgcn_ds_read_tr16_b64_v4i16((LAS s16x4*)(ad[1] + ks * 5120 + 32 * n));
            const u32x2 a = __builtin_bit_cast(u32x2, lo), b = __builtin_bit_cast(u32x2, hi);
            st128(rws, dst + (ks * 5 + n) * 1024 + lane * 16, (u32x4){a[0], a[1], b[0], b[1]});
        }
    asm volatile("s_waitcnt lgkmcnt(0)" ::: "memory");
}

template <int MODE>
__device__ __forceinline__ void phase_t2(const Args& a, unsigned lds0, int w, int lane, rsrc_t rws, Tag T, const Code& code) {
    const T2Tile tile;
    const int tid = threadIdx.x;
    if (w >= 4) {
        const T2Loader<MODE> L(a, lds0, w, lane);
        BARRIER();
        L.run([&](int c) {
        });
        BARRIER();
        BARRIER();
        if (MODE == 0) {
            BARRIER();
            {
                const T1Tile<1> nt;
                if (nt.active) w1_preload(a, lds0, w - 4, lane, nt.cb);
            }
        }
        return;
    }
    const int rh = w >> 1, kg = w & 1, r16 = lane & 15, kq = lane >> 4;
    const int m0 = tile.rb * 64, n0 = tile.cb * 80;
    const int mrow = m0 + 32 * rh + 16 * kg + r16;
    if (w == 1) code_touch(code, lds0, lane);
    if (w == 0) {
        if (MODE == 0) wait_flags(rws, WS_F2 + ((tile.rb >> 2) * 32 + (lane & 31)) * FL, T);
        else           wait_flags(rws, WS_F4 + ((tile.rb >> 1) * 16 + (lane < 14 ? lane : 0)) * FL, T);
    }
    BARRIER();
    const unsigned hbase = WS_H + ((mrow >> 5) * 80 + 10 * tile.cb) * 512 + (mrow & 31) * 16;
    u32x2 resv[5], biasv[5];
#pragma unroll
    for (int n = 0; n < 5; ++n) {
        if (MODE == 0) resv[n] = *(const u32x2*)((const char*)a.x + mrow * (D_ * 2) + (n0 + 16 * n + 4 * kq) * 2);
        else { resv[n] = ld64(rws, hbase + (2 * n + (kq >> 1)) * 512 + (kq & 1) * 8); biasv[n] = *(const u32x2*)((const char*)a.b2 + (n0 + 16 * n + 4 * kq) * 2); }
    }
    f32x4 acc0[5], acc1[5];
    t2_consume<MODE>(lds0, w, lane, acc0, acc1);
    LGKM0_BARRIER();
    LAS f32x4* xch = (LAS f32x4*)lds0;
#pragma unroll
    for (int n = 0; n < 5; ++n) xch[(w * 5 + n) * 64 + lane] = acc1[n];
    LGKM0_BARRIER();
    float s1 = 0.f, s2 = 0.f;
    u32x2 pk[5];
#pragma unroll
    for (int n = 0; n < 5; ++n) {
        const f32x4 v = acc0[n] + xch[((w ^ 1) * 5 + n) * 64 + lane];
        float r[4] = {v[0] + bf_lo(resv[n][0]), v[1] + bf_hi(resv[n][0]), v[2] + bf_lo(resv[n][1]), v[3] + bf_hi(resv[n][1])};
        if (MODE == 1) { r[0] += bf_lo(biasv[n][0]); r[1] += bf_hi(biasv[n][0]); r[2] += bf_lo(biasv[n][1]); r[3] += bf_hi(biasv[n][1]); }
        pk[n] = (u32x2){pk_bf16(r[0], r[1]), pk_bf16(r[2], r[3])};
        if (MODE == 0) {
            const float h0 = bf_lo(pk[n][0]), h1 = bf_hi(pk[n][0]), h2 = bf_lo(pk[n][1]), h3 = bf_hi(pk[n][1]);
            s1 += (h0 + h1) + (h2 + h3);
            s2 += (h0 * h0 + h1 * h1) + (h2 * h2 + h3 * h3);
        }
    }
#pragma unroll
    for (int n = 0; n < 5; n += 2) {
        const int m = n < 4 ? n + 1 : n;
        const auto x = __builtin_amdgcn_permlane16_swap(pk[n][0], pk[m][0], false, false), y = __builtin_amdgcn_permlane16_swap(pk[n][1], pk[m][1], false, false);
        const u32x4 v = {x[0], y[0], x[1], y[1]};
        const int ch = 2 * n + (n < 4 ? 2 * (kq & 1) : 0) + (kq >> 1);
        if (n < 4 || (kq & 1) == 0) {
            if (MODE == 0) st128(rws, hbase + ch * 512, v);
            else __builtin_amdgcn_raw_buffer_store_b128(v, mk_rsrc(a.out, S_ * D_ * 2), mrow * (D_ * 2) + n0 * 2 + ch * 16, 0, 16);
        }
    }
    if (MODE == 0) {
        s1 += __shfl_xor(s1, 16); s2 += __shfl_xor(s2, 16);
        s1 += __shfl_xor(s1, 32); s2 += __shfl_xor(s2, 32);
        if (kq == 0) st64(rws, WS_ST + (tile.cb * S_ + mrow) * 8, (u32x2){__float_as_uint(s1), __float_as_uint(s2)});
        WAIT_VM(0);
        BARRIER();
        if (tid == 0) set_flag(rws, WS_F3 + (tile.rb * 8 + tile.cb) * FL, T);
    } else {
    }
}

#define ATTN_LOOP_ASM \
    "Lattn_chunk%=:\n\t" \
    "s_setprio 2\n\t" \
    "v_exp_f32 v64, v64\n\t" \
    "v_exp_f32 v65, v65\n\t" \
    "v_cvt_pk_bf16_f32 v194, v92, v93\n\t" \
    "v_exp_f32 v66, v66\n\t" \
    "v_cvt_pk_bf16_f32 v195, v94, v95\n\t" \
    "v_exp_f32 v67, v67\n\t" \
    "v_mfma_f32_32x32x16_bf16 v[80:95], v[172:175], v[164:167], v[144:159]\n\t" \
    "v_exp_f32 v68, v68\n\t" \
    "v_cvt_pk_bf16_f32 v188, v64, v65\n\t" \
    "v_exp_f32 v69, v69\n\t" \
    "v_cvt_pk_bf16_f32 v189, v66, v67\n\t" \
    "v_exp_f32 v70, v70\n\t" \
    "v_exp_f32 v71, v71\n\t" \
    "v_mfma_f32_32x32x8_bf16 v[80:95], v[176:177], v[170:171], v[80:95]\n\t" \
    "ds_read_b128 v[172:175], v196 offset:5632\n\t" \
    "ds_read_b64 v[176:177], v197 offset:5632\n\t" \
    "v_cvt_pk_bf16_f32 v190, v68, v69\n\t" \
    "v_exp_f32 v72, v72\n\t" \
    "v_cvt_pk_bf16_f32 v191, v70, v71\n\t" \
    "v_exp_f32 v73, v73\n\t" \
    "v_exp_f32 v74, v74\n\t" \
    "v_exp_f32 v75, v75\n\t" \
    "v_mfma_f32_32x32x16_bf16 v[112:127], v[184:187], v[192:195], v[112:127]\n\t" \
    "ds_read_b128 v[184:187], v203 offset:0\n\t" \
    "v_exp_f32 v76, v76\n\t" \
    "v_cvt_pk_bf16_f32 v192, v72, v73\n\t" \
    "v_exp_f32 v77, v77\n\t" \
    "v_cvt_pk_bf16_f32 v193, v74, v75\n\t" \
    "v_exp_f32 v78, v78\n\t" \
    "v_exp_f32 v79, v79\n\t" \
    "s_waitcnt lgkmcnt(3)\n\t" \
    "v_mfma_f32_32x32x16_bf16 v[96:111], v[180:183], v[188:191], v[96:111]\n\t" \
    "v_exp_f32 v80, v80\n\t" \
    "v_exp_f32 v81, v81\n\t" \
    "v_cvt_pk_bf16_f32 v194, v76, v77\n\t" \
    "v_exp_f32 v82, v82\n\t" \
    "v_cvt_pk_bf16_f32 v195, v78, v79\n\t" \
    "v_exp_f32 v83, v83\n\t" \
    "s_waitcnt lgkmcnt(0)\n\t" \
    "v_mfma_f32_32x32x16_bf16 v[64:79], v[172:175], v[160:163], v[128:143]\n\t" \
    "v_exp_f32 v84, v84\n\t" \
    "v_cvt_pk_bf16_f32 v188, v80, v81\n\t" \
    "v_exp_f32 v85, v85\n\t" \
    "v_cvt_pk_bf16_f32 v189, v82, v83\n\t" \
    "v_exp_f32 v86, v86\n\t" \
    "v_exp_f32 v87, v87\n\t" \
    "v_mfma_f32_32x32x8_bf16 v[64:79], v[176:177], v[168:169], v[64:79]\n\t" \
    "v_cvt_pk_bf16_f32 v190, v84, v85\n\t" \
    "v_exp_f32 v88, v88\n\t" \
    "v_cvt_pk_bf16_f32 v191, v86, v87\n\t" \
    "v_exp_f32 v89, v89\n\t" \
    "v_exp_f32 v90, v90\n\t" \
    "v_exp_f32 v91, v91\n\t" \
    "v_mfma_f32_32x32x16_bf16 v[96:111], v[184:187], v[192:195], v[96:111]\n\t" \
    "v_exp_f32 v92, v92\n\t" \
    "v_cvt_pk_bf16_f32 v192, v88, v89\n\t" \
    "v_exp_f32 v93, v93\n\t" \
    "v_cvt_pk_bf16_f32 v193, v90, v91\n\t" \
    "v_exp_f32 v94, v94\n\t" \
    "v_exp_f32 v95, v95\n\t" \
    "v_mfma_f32_32x32x16_bf16 v[112:127], v[180:183], v[188:191], v[112:127]\n\t" \
    "ds_read_b128 v[180:183], v198 offset:5632\n\t" \
    "s_setprio 1\n\t" \
    "v_exp_f32 v64, v64\n\t" \
    "v_exp_f32 v65, v65\n\t" \
    "v_cvt_pk_bf16_f32 v194, v92, v93\n\t" \
    "v_exp_f32 v66, v66\n\t" \
    "v_cvt_pk_bf16_f32 v195, v94, v95\n\t" \
    "v_exp_f32 v67, v67\n\t" \
    "v_mfma_f32_32x32x16_bf16 v[80:95], v[172:175], v[164:167], v[144:159]\n\t" \
    "v_exp_f32 v68, v68\n\t" \
    "v_cvt_pk_bf16_f32 v188, v64, v65\n\t" \
    "v_exp_f32 v69, v69\n\t" \
    "v_cvt_pk_bf16_f32 v189, v66, v67\n\t" \
    "v_exp_f32 v70, v70\n\t" \
    "v_exp_f32 v71, v71\n\t" \
    "v_mfma_f32_32x32x8_bf16 v[80:95], v[176:177], v[170:171], v[80:95]\n\t" \
    "ds_read_b128 v[172:175], v196 offset:11264\n\t" \
    "ds_read_b64 v[176:177], v197 offset:11264\n\t" \
    "v_cvt_pk_bf16_f32 v190, v68, v69\n\t" \
    "v_exp_f32 v72, v72\n\t" \
    "v_cvt_pk_bf16_f32 v191, v70, v71\n\t" \
    "v_exp_f32 v73, v73\n\t" \
    "v_exp_f32 v74, v74\n\t" \
    "v_exp_f32 v75, v75\n\t" \
    "v_mfma_f32_32x32x16_bf16 v[112:127], v[184:187], v[192:195], v[112:127]\n\t" \
    "ds_read_b128 v[184:187], v203 offset:5632\n\t" \
    "v_exp_f32 v76, v76\n\t" \
    "v_cvt_pk_bf16_f32 v192, v72, v73\n\t" \
    "v_exp_f32 v77, v77\n\t" \
    "v_cvt_pk_bf16_f32 v193, v74, v75\n\t" \
    "v_exp_f32 v78, v78\n\t" \
    "v_exp_f32 v79, v79\n\t" \
    "s_waitcnt lgkmcnt(3)\n\t" \
    "v_mfma_f32_32x32x16_bf16 v[96:111], v[180:183], v[188:191], v[96:111]\n\t" \
    "v_exp_f32 v80, v80\n\t" \
    "v_exp_f32 v81, v81\n\t" \
    "v_cvt_pk_bf16_f32 v194, v76, v77\n\t" \
    "v_exp_f32 v82, v82\n\t" \
    "v_cvt_pk_bf16_f32 v195, v78, v79\n\t" \
    "v_exp_f32 v83, v83\n\t" \
    "s_waitcnt lgkmcnt(0)\n\t" \
    "v_mfma_f32_32x32x16_bf16 v[64:79], v[172:175], v[160:163], v[128:143]\n\t" \
    "v_exp_f32 v84, v84\n\t" \
    "v_cvt_pk_bf16_f32 v188, v80, v81\n\t" \
    "v_exp_f32 v85, v85\n\t" \
    "v_cvt_pk_bf16_f32 v189, v82, v83\n\t" \
    "v_exp_f32 v86, v86\n\t" \
    "v_exp_f32 v87, v87\n\t" \
    "v_mfma_f32_32x32x8_bf16 v[64:79], v[176:177], v[168:169], v[64:79]\n\t" \
    "v_cvt_pk_bf16_f32 v190, v84, v85\n\t" \
    "v_exp_f32 v88, v88\n\t" \
    "v_cvt_pk_bf16_f32 v191, v86, v87\n\t" \
    "v_exp_f32 v89, v89\n\t" \
    "v_exp_f32 v90, v90\n\t" \
    "v_exp_f32 v91, v91\n\t" \
    "v_mfma_f32_32x32x16_bf16 v[96:111], v[184:187], v[192:195], v[96:111]\n\t" \
    "v_exp_f32 v92, v92\n\t" \
    "v_cvt_pk_bf16_f32 v192, v88, v89\n\t" \
    "v_exp_f32 v93, v93\n\t" \
    "v_cvt_pk_bf16_f32 v193, v90, v91\n\t" \
    "v_exp_f32 v94, v94\n\t" \
    "v_exp_f32 v95, v95\n\t" \
    "v_mfma_f32_32x32x16_bf16 v[112:127], v[180:183], v[188:191], v[112:127]\n\t" \
    "ds_read_b128 v[180:183], v198 offset:11264\n\t" \
    "s_setprio 0\n\t" \
    "v_exp_f32 v64, v64\n\t" \
    "v_exp_f32 v65, v65\n\t" \
    "v_cvt_pk_bf16_f32 v194, v92, v93\n\t" \
    "v_exp_f32 v66, v66\n\t" \
    "v_cvt_pk_bf16_f32 v195, v94, v95\n\t" \
    "v_exp_f32 v67, v67\n\t" \
    "v_mfma_f32_32x32x16_bf16 v[80:95], v[172:175], v[164:167], v[144:159]\n\t" \
    "v_exp_f32 v68, v68\n\t" \
    "v_cvt_pk_bf16_f32 v188, v64, v65\n\t" \
    "v_exp_f32 v69, v69\n\t" \
    "v_cvt_pk_bf16_f32 v189, v66, v67\n\t" \
    "v_exp_f32 v70, v70\n\t" \
    "v_exp_f32 v71, v71\n\t" \
    "v_mfma_f32_32x32x8_bf16 v[80:95], v[176:177], v[170:171], v[80:95]\n\t" \
    "ds_read_b128 v[172:175], v196 offset:16896\n\t" \
    "ds_read_b64 v[176:177], v197 offset:16896\n\t" \
    "v_cvt_pk_bf16_f32 v190, v68, v69\n\t" \
    "v_exp_f32 v72, v72\n\t" \
    "v_cvt_pk_bf16_f32 v191, v70, v71\n\t" \
    "v_exp_f32 v73, v73\n\t" \
    "v_exp_f32 v74, v74\n\t" \
    "v_exp_f32 v75, v75\n\t" \
    "v_mfma_f32_32x32x16_bf16 v[112:127], v[184:187], v[192:195], v[112:127]\n\t" \
    "ds_read_b128 v[184:187], v203 offset:11264\n\t" \
    "v_exp_f32 v76, v76\n\t" \
    "v_cvt_pk_bf16_f32 v192, v72, v73\n\t" \
    "v_exp_f32 v77, v77\n\t" \
    "v_cvt_pk_bf16_f32 v193, v74, v75\n\t" \
    "v_exp_f32 v78, v78\n\t" \
    "v_exp_f32 v79, v79\n\t" \
    "s_waitcnt lgkmcnt(3)\n\t" \
    "v_mfma_f32_32x32x16_bf16 v[96:111], v[180:183], v[188:191], v[96:111]\n\t" \
    "v_exp_f32 v80, v80\n\t" \
    "v_exp_f32 v81, v81\n\t" \
    "v_cvt_pk_bf16_f32 v194, v76, v77\n\t" \
    "v_exp_f32 v82, v82\n\t" \
    "v_cvt_pk_bf16_f32 v195, v78, v79\n\t" \
    "v_exp_f32 v83, v83\n\t" \
    "s_waitcnt lgkmcnt(0)\n\t" \
    "v_mfma_f32_32x32x16_bf16 v[64:79], v[172:175], v[160:163], v[128:143]\n\t" \
    "v_exp_f32 v84, v84\n\t" \
    "v_cvt_pk_bf16_f32 v188, v80, v81\n\t" \
    "v_exp_f32 v85, v85\n\t" \
    "v_cvt_pk_bf16_f32 v189, v82, v83\n\t" \
    "v_exp_f32 v86, v86\n\t" \
    "v_exp_f32 v87, v87\n\t" \
    "v_mfma_f32_32x32x8_bf16 v[64:79], v[176:177], v[168:169], v[64:79]\n\t" \
    "v_cvt_pk_bf16_f32 v190, v84, v85\n\t" \
    "v_exp_f32 v88, v88\n\t" \
    "v_cvt_pk_bf16_f32 v191, v86, v87\n\t" \
    "v_exp_f32 v89, v89\n\t" \
    "v_exp_f32 v90, v90\n\t" \
    "v_exp_f32 v91, v91\n\t" \
    "v_mfma_f32_32x32x16_bf16 v[96:111], v[184:187], v[192:195], v[96:111]\n\t" \
    "v_exp_f32 v92, v92\n\t" \
    "v_cvt_pk_bf16_f32 v192, v88, v89\n\t" \
    "v_exp_f32 v93, v93\n\t" \
    "v_cvt_pk_bf16_f32 v193, v90, v91\n\t" \
    "v_exp_f32 v94, v94\n\t" \
    "v_exp_f32 v95, v95\n\t" \
    "v_mfma_f32_32x32x16_bf16 v[112:127], v[180:183], v[188:191], v[112:127]\n\t" \
    "ds_read_b128 v[180:183], v198 offset:16896\n\t" \
    "s_sub_u32 %[tmp], %[cnt], 1\n\t" \
    "s_cmp_lt_u32 %[tmp], 5\n\t" \
    "s_cbranch_scc1 Lattn_w3%=\n\t" \
    "s_waitcnt vmcnt(1)\n\t" \
    "Lattn_w3%=:\n\t" \
    "s_waitcnt vmcnt(3)\n\t" \
    "s_barrier\n\t" \
    "s_cmp_ge_u32 %[cnt], 6\n\t" \
    "s_cbranch_scc1 Lattn_nodma%=\n\t" \
    "s_add_u32 m0, %[slot], %[p0]\n\t" \
    "s_add_u32 %[tmp], %[src], %[p0]\n\t" \
    "buffer_load_dwordx4 v202, %[rsrc], %[tmp] offen sc1 lds\n\t" \
    "s_add_u32 m0, %[slot], %[p1]\n\t" \
    "s_add_u32 %[tmp], %[src], %[p1]\n\t" \
    "buffer_load_dwordx4 v202, %[rsrc], %[tmp] offen sc1 lds\n\t" \
    "s_add_u32 m0, %[slot], %[p2]\n\t" \
    "s_add_u32 %[tmp], %[src], %[p2]\n\t" \
    "buffer_load_dwordx4 v202, %[rsrc], %[tmp] offen sc1 lds\n\t" \
    "s_mov_b32 m0, %[scr]\n\t" \
    "v_add_co_u32 v206, vcc, v204, v206\n\t" \
    "global_load_lds_dwordx4 v[208:209], off\n\t" \
    "s_add_u32 %[src], %[src], 22528\n\t" \
    "s_nop 0\n\t" \
    "v_addc_co_u32 v207, vcc, 0, v207, vcc\n\t" \
    "v_mov_b32 v208, v206\n\t" \
    "v_mov_b32 v209, v207\n\t" \
    "s_cmp_ge_u32 %[cnt], 5\n\t" \
    "s_cbranch_scc1 Lattn_nowt%=\n\t" \
    "s_mov_b32 m0, %[wtl]\n\t" \
    "s_add_u32 %[wtl], %[wtl], 16384\n\t" \
    "buffer_load_dwordx4 v202, %[rwt], %[wts] offen sc1 lds\n\t" \
    "buffer_load_dwordx4 v202, %[rwt], %[wts] offen offset:1024 sc1 lds\n\t" \
    "s_add_u32 %[wts], %[wts], 16384\n\t" \
    "Lattn_nowt%=:\n\t" \
    "s_add_u32 %[slot], %[slot], 22528\n\t" \
    "s_cmp_eq_u32 %[slot], %[rend]\n\t" \
    "s_cselect_b32 %[slot], %[lds0], %[slot]\n\t" \
    "Lattn_nodma%=:\n\t" \
    "s_setprio 3\n\t" \
    "v_exp_f32 v64, v64\n\t" \
    "v_exp_f32 v65, v65\n\t" \
    "v_cvt_pk_bf16_f32 v194, v92, v93\n\t" \
    "v_exp_f32 v66, v66\n\t" \
    "v_cvt_pk_bf16_f32 v195, v94, v95\n\t" \
    "v_exp_f32 v67, v67\n\t" \
    "v_mfma_f32_32x32x16_bf16 v[80:95], v[172:175], v[164:167], v[144:159]\n\t" \
    "v_exp_f32 v68, v68\n\t" \
    "v_cvt_pk_bf16_f32 v188, v64, v65\n\t" \
    "v_exp_f32 v69, v69\n\t" \
    "v_cvt_pk_bf16_f32 v189, v66, v67\n\t" \
    "v_exp_f32 v70, v70\n\t" \
    "v_exp_f32 v71, v71\n\t" \
    "v_mfma_f32_32x32x8_bf16 v[80:95], v[176:177], v[170:171], v[80:95]\n\t" \
    "ds_read_b128 v[172:175], v199 offset:0\n\t" \
    "ds_read_b64 v[176:177], v200 offset:0\n\t" \
    "v_cvt_pk_bf16_f32 v190, v68, v69\n\t" \
    "v_exp_f32 v72, v72\n\t" \
    "v_cvt_pk_bf16_f32 v191, v70, v71\n\t" \
    "v_exp_f32 v73, v73\n\t" \
    "v_exp_f32 v74, v74\n\t" \
    "v_exp_f32 v75, v75\n\t" \
    "v_mfma_f32_32x32x16_bf16 v[112:127], v[184:187], v[192:195], v[112:127]\n\t" \
    "ds_read_b128 v[184:187], v203 offset:16896\n\t" \
    "v_exp_f32 v76, v76\n\t" \
    "v_cvt_pk_bf16_f32 v192, v72, v73\n\t" \
    "v_exp_f32 v77, v77\n\t" \
    "v_cvt_pk_bf16_f32 v193, v74, v75\n\t" \
    "v_exp_f32 v78, v78\n\t" \
    "v_exp_f32 v79, v79\n\t" \
    "s_waitcnt lgkmcnt(3)\n\t" \
    "v_mfma_f32_32x32x16_bf16 v[96:111], v[180:183], v[188:191], v[96:111]\n\t" \
    "v_exp_f32 v80, v80\n\t" \
    "v_exp_f32 v81, v81\n\t" \
    "v_cvt_pk_bf16_f32 v194, v76, v77\n\t" \
    "v_exp_f32 v82, v82\n\t" \
    "v_cvt_pk_bf16_f32 v195, v78, v79\n\t" \
    "v_exp_f32 v83, v83\n\t" \
    "s_waitcnt lgkmcnt(0)\n\t" \
    "v_mfma_f32_32x32x16_bf16 v[64:79], v[172:175], v[160:163], v[128:143]\n\t" \
    "v_exp_f32 v84, v84\n\t" \
    "v_cvt_pk_bf16_f32 v188, v80, v81\n\t" \
    "v_exp_f32 v85, v85\n\t" \
    "v_cvt_pk_bf16_f32 v189, v82, v83\n\t" \
    "v_exp_f32 v86, v86\n\t" \
    "v_exp_f32 v87, v87\n\t" \
    "v_mfma_f32_32x32x8_bf16 v[64:79], v[176:177], v[168:169], v[64:79]\n\t" \
    "v_cvt_pk_bf16_f32 v190, v84, v85\n\t" \
    "v_exp_f32 v88, v88\n\t" \
    "v_cvt_pk_bf16_f32 v191, v86, v87\n\t" \
    "v_exp_f32 v89, v89\n\t" \
    "v_exp_f32 v90, v90\n\t" \
    "v_exp_f32 v91, v91\n\t" \
    "v_mfma_f32_32x32x16_bf16 v[96:111], v[184:187], v[192:195], v[96:111]\n\t" \
    "v_exp_f32 v92, v92\n\t" \
    "v_cvt_pk_bf16_f32 v192, v88, v89\n\t" \
    "v_exp_f32 v93, v93\n\t" \
    "v_cvt_pk_bf16_f32 v193, v90, v91\n\t" \
    "v_exp_f32 v94, v94\n\t" \
    "v_exp_f32 v95, v95\n\t" \
    "v_mfma_f32_32x32x16_bf16 v[112:127], v[180:183], v[188:191], v[112:127]\n\t" \
    "ds_read_b128 v[180:183], v201 offset:0\n\t" \
    "v_mov_b32 v196, v199\n\t" \
    "v_mov_b32 v197, v200\n\t" \
    "v_mov_b32 v198, v201\n\t" \
    "v_xor_b32 v203, 128, v201\n\t" \
    "s_add_u32 %[tmp], %[nxt], 22528\n\t" \
    "s_cmp_eq_u32 %[tmp], %[rend]\n\t" \
    "s_cselect_b32 %[tmp], %[lds0], %[tmp]\n\t" \
    "s_sub_u32 %[dlt], %[tmp], %[nxt]\n\t" \
    "s_mov_b32 %[nxt], %[tmp]\n\t" \
    "v_add_u32 v199, %[dlt], v199\n\t" \
    "v_add_u32 v200, %[dlt], v200\n\t" \
    "v_add_u32 v201, %[dlt], v201\n\t" \
    "s_add_u32 %[cnt], %[cnt], 1\n\t" \
    "s_cmp_lt_u32 %[cnt], 8\n\t" \
    "s_cbranch_scc1 Lattn_chunk%=\n\t" \
    "v_cvt_pk_bf16_f32 v194, v92, v93\n\t" \
    "v_cvt_pk_bf16_f32 v195, v94, v95\n\t" \
    "s_nop 1\n\t" \
    "v_mfma_f32_32x32x16_bf16 v[112:127], v[184:187], v[192:195], v[112:127]\n\t" \
    "s_setprio 0\n\t" \
    "s_waitcnt lgkmcnt(0)\n\t" \
    "s_nop 15\n\t" \
    ""

struct AttnCtx { unsigned lds0, kvbase, p0, p1, p2, vsrc, oK0, oK1, oV; rsrc_t rKV; };
__device__ __forceinline__ void attn_issue(const AttnCtx& c, int chunk, int slot) {
    const unsigned l = c.lds0 + slot * AT_CHUNK, g = c.kvbase + chunk * AT_CHUNK;
    dma16<true>(l + c.p0, c.vsrc, c.rKV, g + c.p0);
    dma16<true>(l + c.p1, c.vsrc, c.rKV, g + c.p1);
    dma16<true>(l + c.p2, c.vsrc, c.rKV, g + c.p2);
}
__device__ __forceinline__ float rowmax16(const f32x16& s) {
    float mx = s[0];
#pragma unroll
    for (int i = 1; i < 16; ++i) mx = __builtin_fmaxf(mx, s[i]);
    return swap32_max(mx);
}
__device__ __forceinline__ void attn_safe(const AttnCtx& c, int g, bf16x8 qX0, s16x4 qX1, bf16x8 qY0, s16x4 qY1,
                                          f32x16& oX, f32x16& oY, float& mX, float& mY) {
    oX = f32x16{}; oY = f32x16{}; mX = -INFINITY; mY = -INFINITY;
#pragma unroll 1
    for (int ch = 0; ch < 8; ++ch) {
        LGKM0_BARRIER();
        attn_issue(c, ch, 0);
        WAIT_VM(0); BARRIER();
#pragma unroll 1
        for (int i = 0; i < 4; ++i) {
            const unsigned tb = c.lds0 + (2 * i + g) * KV_TILE;
            const bf16x8 k0 = lds_rd128(tb + c.oK0), v0 = lds_rd128(tb + c.oV), v1 = lds_rd128(tb + (c.oV ^ 128));
            const s16x4 k1 = *(LAS const s16x4*)(tb + c.oK1);
#pragma unroll
            for (int z = 0; z < 2; ++z) {
                f32x16& o = z ? oY : oX; float& m = z ? mY : mX;
                f32x16 s = MFMA32(k0, z ? qY0 : qX0, f32x16{});
                s = MFMA32K8(k1, z ? qY1 : qX1, s);
                const float mx = __builtin_fmaxf(rowmax16(s), m);
                const float alpha = __builtin_amdgcn_exp2f(m - mx);
                m = mx;
                u32x4 p0, p1;
#pragma unroll
                for (int r = 0; r < 16; ++r) o[r] *= alpha;
#pragma unroll
                for (int r = 0; r < 4; ++r) {
                    p0[r] = pk_bf16(__builtin_amdgcn_exp2f(s[2 * r] - m), __builtin_amdgcn_exp2f(s[2 * r + 1] - m));
                    p1[r] = pk_bf16(__builtin_amdgcn_exp2f(s[8 + 2 * r] - m), __builtin_amdgcn_exp2f(s[8 + 2 * r + 1] - m));
                }
                o = MFMA32(v0, __builtin_bit_cast(bf16x8, p0), o);
                o = MFMA32(v1, __builtin_bit_cast(bf16x8, p1), o);
            }
        }
    }
}
__device__ __forceinline__ void attn_merge(unsigned lds0, int w, int lane, const f32x16& oX, const f32x16& oY, float mX, float mY, float (&o)[12],
                                           bool bad, LAS int* flag) {
    LAS f32x4* xq = (LAS f32x4*)lds0;
    LAS float* xm = (LAS float*)(lds0 + 8 * 3 * 1024);
    LGKM0_BARRIER();
#pragma unroll
    for (int r = 0; r < 3; ++r) xq[(w * 3 + r) * 64 + lane] = (f32x4){oY[4 * r], oY[4 * r + 1], oY[4 * r + 2], oY[4 * r + 3]};
    xm[w * 64 + lane] = mY;
    if (__any(bad) && lane == 0) *flag = 1;
    LGKM0_BARRIER();
    const float mo = xm[(w ^ 1) * 64 + lane];
    const float m = __builtin_fmaxf(mo, mX);
    const float fo = __builtin_amdgcn_exp2f(mo - m), fm = __builtin_amdgcn_exp2f(mX - m);
#pragma unroll
    for (int r = 0; r < 3; ++r) {
        const f32x4 z = xq[((w ^ 1) * 3 + r) * 64 + lane];
#pragma unroll
        for (int e = 0; e < 4; ++e) o[4 * r + e] = __builtin_fmaf(oX[4 * r + e], fm, z[e] * fo);
    }
}

__device__ __forceinline__ void phase_attn(const Args& a, unsigned lds0, int w, int lane, rsrc_t rws, Tag T, const Code& code) {
    const int tid = threadIdx.x, r32 = lane & 31, hi = lane >> 5;
    const int xcd = blockIdx.x & 7, j = blockIdx.x >> 3;
    const int head = xcd * 4 + (j & 3), qb = j >> 2;
    const int qg = w >> 1, g = w & 1;
    if (w == 1) code_touch(code, lds0, lane);
    if (w == 0) {
        const int c0 = (head * 20) >> 7, c1 = (head * 20 + 19) >> 7;
        wait_flags3(rws, WS_F1 + (((hi ? 10 : 5) + ((lane & 16) ? c1 : c0)) * 16 + (lane & 15)) * FL,
                    WS_F1 + (((lane & 2) ? c1 : c0) * 16 + 2 * qb + (lane & 1)) * FL,
                    T2Loader<0>::fw_off(lane), T);
    }
    BARRIER();
    AttnCtx c;
    c.lds0 = lds0; c.kvbase = head * 64 * KV_TILE; c.rKV = mk_rsrc(a.ws + WS_KV, H_ * 64 * KV_TILE);
    c.p0 = w * 1024; c.p1 = (w + 8) * 1024; c.p2 = (w < 6 ? w + 16 : w + 8) * 1024;
    c.vsrc = lane * 16;
    c.oK0 = lane * 16; c.oK1 = hi ? v_off(21, 0, 0) : 1024 + r32 * 8;
    c.oV = v_off(r32 <= 21 ? r32 : 21, 0, hi);
    const char* qp = a.ws + WS_Q + (size_t)(head * 64 + qb * 8 + qg * 2 + g) * QK_TILE;
    const char* qp2 = qp + (g ? -QK_TILE : QK_TILE);
    bf16x8 qX0, qY0; s16x4 qX1, qY1;
    {
        const unsigned o0 = lane * 16, o1 = 1024 + r32 * 8;
        asm volatile("global_load_dwordx4 %0, %4, %6 sc1\n\tglobal_load_dwordx4 %1, %4, %7 sc1\n\t"
                     "global_load_dwordx2 %2, %5, %6 sc1\n\tglobal_load_dwordx2 %3, %5, %7 sc1"
                     : "=&{v[160:163]}"(qX0), "=&{v[164:167]}"(qY0), "=&{v[168:169]}"(qX1), "=&{v[170:171]}"(qY1) : "v"(o0), "v"(o1), "s"(qp), "s"(qp2) : "memory");
    }
    attn_issue(c, 0, 0); attn_issue(c, 1, 1);
    asm volatile("s_waitcnt vmcnt(3)\n\ts_barrier" : "+{v[160:163]}"(qX0), "+{v[164:167]}"(qY0), "+{v[168:169]}"(qX1), "+{v[170:171]}"(qY1) :: "memory");
    if (hi) { qX1 = s16x4{0, 0, 0, 0}; qY1 = s16x4{0, 0, 0, 0}; }
    u64 wadr; unsigned wstep;
    {
        const int wi = j * 8 + w, gw = blockIdx.x * 8 + w;
        const int l2 = (gw * 12 < 17280 - 12 ? gw * 12 : 17280 - 12) + (lane == 14 ? 0 : 1);
        const bool isw1 = lane < 14 && wi < 107;
        const int row = wi * 6 < 634 ? wi * 6 : 634;
        int col = (xcd & 1) * 1792 + lane * 128; if (col > F_ * 2 - 128) col = F_ * 2 - 128;
        wadr = isw1 ? (u64)a.w1 + row * (F_ * 2) + col : (u64)a.w2 + l2 * 128;
        wstep = isw1 ? F_ * 2 : 256;
        touch(lds0, mk_rsrc(a.w1, 0), 0u);
    }
    LAS int* flag = (LAS int*)(lds0 + L_AUX);
    if (tid == 0) *flag = 0;
    f32x16 nX, nY, sX, sY, oX = f32x16{}, oY = f32x16{};
    float mX, mY;
    {
        const bf16x8 k0 = lds_rd128(lds0 + c.oK0);
        const s16x4 k1 = *(LAS const s16x4*)(lds0 + c.oK1);
        sX = MFMA32K8(k1, qX1, MFMA32(k0, qX0, f32x16{}));
        sY = MFMA32K8(k1, qY1, MFMA32(k0, qY0, f32x16{}));
        mX = rowmax16(sX); mY = rowmax16(sY);
#pragma unroll
        for (int r = 0; r < 16; ++r) { nX[r] = -mX; nY[r] = -mY; }
    }
    {
        const unsigned tb = lds0 + g * KV_TILE;
        bf16x8 k0 = lds_rd128(tb + c.oK0), v0 = lds_rd128(tb + c.oV), v1 = bf16x8{};
        s16x4 k1 = *(LAS const s16x4*)(tb + c.oK1);
        sX = MFMA32K8(k1, qX1, MFMA32(k0, qX0, nX));
        u32x4 pl = {0, 0, 0, 0}, ph = {0, 0, 0, 0};
        unsigned aK0c = tb + c.oK0, aK1c = tb + c.oK1, aVc = tb + c.oV, aV1c = tb + (c.oV ^ 128);
        unsigned aK0n = aK0c + AT_CHUNK, aK1n = aK1c + AT_CHUNK, aVn = aVc + AT_CHUNK;
        u64 wadr0 = wadr;
        const T2Tile nt;
        unsigned wtl = lds0 + T2_B + 20480 + w * 2048, wts = nt.cb * 102400 + 20480 + w * 2048;
        const rsrc_t rwt = mk_rsrc(a.ws + WS_WT0, 8 * 10 * 10240);
        unsigned cnt = 0, slot = lds0 + 2 * AT_CHUNK, src = c.kvbase + 2 * AT_CHUNK, nxt = lds0 + AT_CHUNK, tmp, dlt;
        asm volatile("s_waitcnt lgkmcnt(0)\n\ts_nop 15\n\t" ATTN_LOOP_ASM
            : "+{v[64:79]}"(sX), "+{v[80:95]}"(sY), "+{v[96:111]}"(oX), "+{v[112:127]}"(oY),
              "+{v[172:175]}"(k0), "+{v[176:177]}"(k1), "+{v[180:183]}"(v0), "+{v[184:187]}"(v1),
              "+{v[188:191]}"(pl), "+{v[192:195]}"(ph),
              "+{v196}"(aK0c), "+{v197}"(aK1c), "+{v198}"(aVc), "+{v199}"(aK0n), "+{v200}"(aK1n), "+{v201}"(aVn), "+{v203}"(aV1c),
              "+{v[206:207]}"(wadr), "+{v[208:209]}"(wadr0),
              [wtl] "+&s"(wtl), [wts] "+&s"(wts), [cnt] "+&s"(cnt), [slot] "+&s"(slot), [src] "+&s"(src), [nxt] "+&s"(nxt), [tmp] "=&s"(tmp), [dlt] "=&s"(dlt)
            : "{v[128:143]}"(nX), "{v[144:159]}"(nY), "{v[160:163]}"(qX0), "{v[164:167]}"(qY0), "{v[168:169]}"(qX1), "{v[170:171]}"(qY1),
              "{v202}"(c.vsrc), "{v204}"(wstep), [scr] "s"(lds0 + L_SCR), [rwt] "s"(rwt), [rsrc] "s"(c.rKV), [p0] "s"(c.p0), [p1] "s"(c.p1), [p2] "s"(c.p2), [lds0] "s"(lds0), [rend] "s"(lds0 + AT_RING)
            : "memory", "scc", "vcc");
    }
    bool bad = hi && !(oX[8] <= 5.4975581e11f && oY[8] <= 5.4975581e11f);
    {
        f32x2 z = {0.f, 0.f};
        const f32x2 zero = {0.f, 0.f};
#pragma unroll
        for (int i = 0; i < 12; i += 2) { z = fma2((f32x2){oX[i], oX[i + 1]}, zero, z); z = fma2((f32x2){oY[i], oY[i + 1]}, zero, z); }
        const float t = z[0] + z[1];
        bad = bad || t != t;
    }
    float o[12];
    attn_merge(lds0, w, lane, oX, oY, mX, mY, o, bad, flag);
    if (*flag) {
        attn_safe(c, g, qX0, qX1, qY0, qY1, oX, oY, mX, mY);
        attn_merge(lds0, w, lane, oX, oY, mX, mY, o, false, flag);
        LGKM0_BARRIER();
    }
    const float l = swap32_add(hi ? o[8] : 0.f);
    const float rl = 1.f / l;
    {
        const u32x2 r0 = {pk_bf16(o[0] * rl, o[1] * rl), pk_bf16(o[2] * rl, o[3] * rl)};
        const u32x2 r1 = {pk_bf16(o[4] * rl, o[5] * rl), pk_bf16(o[6] * rl, o[7] * rl)};
        const u32x2 r2 = {pk_bf16(o[8] * rl, o[9] * rl), pk_bf16(o[10] * rl, o[11] * rl)};
        const unsigned base = WS_O + (qb * 8 + qg * 2 + g) * 40960 + (head >> 1) * 2560 + r32 * 16;
        if ((head & 1) == 0) {
            const auto x = __builtin_amdgcn_permlane32_swap(r0[0], r1[0], false, false), y = __builtin_amdgcn_permlane32_swap(r0[1], r1[1], false, false);
            st128(rws, base + hi * 512, (u32x4){x[0], y[0], x[1], y[1]});
            if (!hi) st64(rws, base + 1024, r2);
        } else {
            const auto x = __builtin_amdgcn_permlane32_swap(r1[0], r1[0], false, false), y = __builtin_amdgcn_permlane32_swap(r1[1], r1[1], false, false);
            const u32x4 v = hi ? (u32x4){r0[0], r0[1], x[0], y[0]} : (u32x4){x[1], y[1], r2[0], r2[1]};
            st128(rws, base + (hi ? 1536 : 2048), v);
            if (!hi) st64(rws, base + 1024 + 8, r0);
        }
    }
    WAIT_VM(0);
    BARRIER();
    if (tid == 0) set_flag(rws, WS_F2 + (qb * 32 + head) * FL, T);
    if (w >= 4) wt_preload<0, 0, 2>(a, lds0, w - 4, lane, rws, T, (u32x2){T.lo, T.hi});
}

struct ColSum {
    u32x4 v[20];
    __device__ __forceinline__ void load(const void* W, int ncols, int blk, int tid) {
        const int cc = tid & 15, rg = tid >> 4, col = blk * 128 + cc * 8 < ncols ? blk * 128 + cc * 8 : 0;
        const char* p = (const char*)W + (size_t)rg * (ncols * 2) + col * 2;
#pragma unroll
        for (int r = 0; r < 20; ++r) v[r] = *(const u32x4*)(p + (size_t)r * 32 * (ncols * 2));
    }
    __device__ __forceinline__ void finish(int ncols, int blk, rsrc_t rws, unsigned dst, Tag t, unsigned lds0, int tid) const {
        const int cc = tid & 15, rg = tid >> 4;
        float s[8];
#pragma unroll
        for (int i = 0; i < 8; ++i) s[i] = 0.f;
#pragma unroll
        for (int r = 0; r < 20; ++r)
#pragma unroll
            for (int i = 0; i < 4; ++i) { s[2 * i] += bf_lo(v[r][i]); s[2 * i + 1] += bf_hi(v[r][i]); }
        LAS float* red = (LAS float*)lds0;
#pragma unroll
        for (int i = 0; i < 8; ++i) red[rg * 128 + cc * 8 + i] = s[i];
        LGKM0_BARRIER();
        if (tid < 128 && blk * 128 + tid < ncols) {
            float c = 0.f;
#pragma unroll
            for (int r = 0; r < 32; ++r) c += red[r * 128 + tid];
            st128(rws, dst + (blk * 128 + tid) * 16, (u32x4){__float_as_uint(c), 0u, t.lo, t.hi});
        }
    }
};
__device__ __forceinline__ void x_stats(const Args& a, int w, int lane, rsrc_t rws, Tag& T) {
    const int r32 = lane & 31, hi = lane >> 5;
    const int row = blockIdx.x * 8 + 2 * w + hi;
    const char* xp = (const char*)a.x + (size_t)row * (D_ * 2) + r32 * 8;
    u32x2 v[5];
#pragma unroll
    for (int i = 0; i < 5; ++i) v[i] = *(const u32x2*)(xp + i * 256);
    T = read_tag(a.ws);
    float s = 0.f;
#pragma unroll
    for (int i = 0; i < 5; ++i) s += (bf_lo(v[i][0]) + bf_hi(v[i][0])) + (bf_lo(v[i][1]) + bf_hi(v[i][1]));
#pragma unroll
    for (int m = 1; m < 32; m <<= 1) s += __shfl_xor(s, m);
    const float mean = s * (1.f / D_);
    float q = 0.f;
#pragma unroll
    for (int i = 0; i < 5; ++i) {
        const float d0 = bf_lo(v[i][0]) - mean, d1 = bf_hi(v[i][0]) - mean, d2 = bf_lo(v[i][1]) - mean, d3 = bf_hi(v[i][1]) - mean;
        q += (d0 * d0 + d1 * d1) + (d2 * d2 + d3 * d3);
    }
#pragma unroll
    for (int m = 1; m < 32; m <<= 1) q += __shfl_xor(q, m);
    const float rstd = __builtin_amdgcn_rcpf(bf_round(__builtin_amdgcn_sqrtf(bf_round(bf_round(q * (1.f / D_)) + LN_EPS))));
    if (r32 == 0) st128(rws, WS_XS + row * 16, (u32x4){__float_as_uint(bf_round(mean)), __float_as_uint(rstd), T.lo, T.hi});
}

__device__ __forceinline__ void phase_qkv(const Args& a, unsigned lds0, int w, int lane, rsrc_t rws, Tag& T, const Code& code) {
    const T1Tile<0> tile;
    const int tid = threadIdx.x, r32 = lane & 31, hi = lane >> 5;
    if (__builtin_expect(w >= 4 && tile.active, 1)) {
        const T1Loader<0> L(a, lds0, w, lane, tile.rb, tile.cb);
        L.prologueB();
        L.run([](int) {}, [&]() {
        });
        T = read_tag(a.ws);
        BARRIER();
        BARRIER();
        return;
    }
    if (!tile.active) {
        const int blk = tile.rb, u = blk * 5 + w;
        const unsigned lw = lds0 + 16384 + w * 10240;
        if (w < 5) retile_issue(a.wo, D_, u / 10, u % 10, lw, lane);
        ColSum cs;
        cs.load(a.wqkv, 1920, blk < 15 ? blk : 0, tid);
        if (w < 4) x_stats(a, w, lane, rws, T);
        if (blk < 15) cs.finish(1920, blk, rws, WS_CQ, T, lds0, tid);
        if (w < 5) retile_finish(rws, WS_WT0 + u * 10240, lw, lane);
        WAIT_VM(0);
        if (w >= 4) T = read_tag(a.ws);
        BARRIER();
        if (tid == 0) { set_flag(rws, WS_FW + blk * FL, T); set_flag(rws, WS_F0 + blockIdx.x * FL, T); }
        return;
    }
    x_stats(a, w, lane, rws, T);
    const int m0 = tile.rb * 128, n0 = tile.cb * 128, cb = tile.cb;
    const int wr = w >> 1, wc = w & 1;
    const bool vblk = cb >= 10;
    f32x16 acc[2][2];
    const unsigned goff = hi ? WS_CQ + (n0 + 32 * w + r32) * 16 : WS_XS + (m0 + 32 * w + r32) * 16;
    u32x4 gran;
    t1_consume<0>(lds0, w, lane, vblk, acc, rws, goff, gran);
    LAS float* smean = (LAS float*)(lds0 + L_AUX);
    LAS float* srstd = smean + 128;
    LAS float* scol = smean + 256;
    {
        const f32x2 g = wait_granule(rws, goff, T, gran);
        if (hi) scol[32 * w + r32] = g[0]; else { smean[32 * w + r32] = g[0]; srstd[32 * w + r32] = g[1]; }
    }
    LGKM0_BARRIER();
    if (!vblk) {
        const float sc = cb < 5 ? QSCALE : 1.f;
        const unsigned img = cb < 5 ? WS_Q : WS_KV, tstride = cb < 5 ? QK_TILE : KV_TILE;
        const int nq = n0 - (cb < 5 ? 0 : D_) + 64 * wc;
        f32x4 cs[2][4];
#pragma unroll
        for (int u = 0; u < 2; ++u)
#pragma unroll
            for (int g = 0; g < 4; ++g) cs[u][g] = *(LAS const f32x4*)(scol + 64 * wc + 32 * u + 8 * g + 4 * hi);
#pragma unroll
        for (int t = 0; t < 2; ++t) {
            const int ml = 64 * wr + 32 * t + r32;
            const float rs = srstd[ml] * sc, nm = -smean[ml];
            const f32x2 rs2 = {rs, rs}, nm2 = {nm, nm};
            const unsigned tb = img + ((m0 + 64 * wr + 32 * t) >> 5) * tstride;
#pragma unroll
            for (int u = 0; u < 2; ++u) {
                u32x2 pk[4];
#pragma unroll
                for (int g = 0; g < 4; ++g) {
                    const f32x2 a0 = {acc[t][u][4 * g], acc[t][u][4 * g + 1]}, a1 = {acc[t][u][4 * g + 2], acc[t][u][4 * g + 3]};
                    pk[g] = (u32x2){pk_bf16(fma2(nm2, lo2(cs[u][g]), a0) * rs2), pk_bf16(fma2(nm2, hi2(cs[u][g]), a1) * rs2)};
                }
#pragma unroll
                for (int gp = 0; gp < 2; ++gp) {
                    const auto x = __builtin_amdgcn_permlane32_swap(pk[2 * gp][0], pk[2 * gp + 1][0], false, false);
                    const auto y = __builtin_amdgcn_permlane32_swap(pk[2 * gp][1], pk[2 * gp + 1][1], false, false);
                    unsigned o[2]; bool tail[2];
#pragma unroll
                    for (int h = 0; h < 2; ++h) {
                        const int nf = nq + 32 * u + 16 * gp + 8 * h, pr = (nf * 1639) >> 16, g8 = (nf - pr * 40) >> 3;
                        tail[h] = g8 == 2;
                        o[h] = (2 * pr + (g8 > 2 ? 1 : 0)) * 64 * tstride + (g8 == 2 ? 1024 : (g8 == 1 || g8 == 4) ? 512 : 0);
                    }
                    const unsigned off = tb + (hi ? o[1] : o[0]);
                    if (hi ? tail[1] : tail[0]) {
                        st64(rws, off + r32 * 8, (u32x2){x[0], y[0]});
                        st64(rws, off + 64 * tstride + r32 * 8, (u32x2){x[1], y[1]});
                    } else {
                        st128(rws, off + r32 * 16, (u32x4){x[0], y[0], x[1], y[1]});
                    }
                }
            }
        }
    } else {
        f32x4 mn[2][4], rs[2][4];
#pragma unroll
        for (int t = 0; t < 2; ++t)
#pragma unroll
            for (int g = 0; g < 4; ++g) {
                mn[t][g] = *(LAS const f32x4*)(smean + 64 * wr + 32 * t + 8 * g + 4 * hi);
                rs[t][g] = *(LAS const f32x4*)(srstd + 64 * wr + 32 * t + 8 * g + 4 * hi);
            }
#pragma unroll
        for (int u = 0; u < 2; ++u) {
            const int nl = 64 * wc + 32 * u + r32;
            const float ncs = -scol[nl];
            const f32x2 nc2 = {ncs, ncs};
            const int nn = n0 + nl - 2 * D_;
            const int head = (nn * 3277) >> 16, d = nn - head * 20;
#pragma unroll
            for (int t = 0; t < 2; ++t) {
                const unsigned base = WS_KV + (head * 64 + ((m0 + 64 * wr + 32 * t) >> 5)) * KV_TILE;
#pragma unroll
                for (int s = 0; s < 2; ++s) {
                    u32x4 o;
#pragma unroll
                    for (int g = 0; g < 2; ++g) {
                        const int r = 8 * s + 4 * g, q = 2 * s + g;
                        const f32x2 a0 = {acc[t][u][r], acc[t][u][r + 1]}, a1 = {acc[t][u][r + 2], acc[t][u][r + 3]};
                        o[2 * g] = pk_bf16(fma2(nc2, lo2(mn[t][q]), a0) * lo2(rs[t][q]));
                        o[2 * g + 1] = pk_bf16(fma2(nc2, hi2(mn[t][q]), a1) * hi2(rs[t][q]));
                    }
                    st128(rws, base + v_off(d, s, hi), o);
                    if (d < 4) { const unsigned cv = d == 0 ? 0x3f803f80u : 0u; st128(rws, base + v_off(20 + d, s, hi), (u32x4){cv, cv, cv, cv}); }
                }
            }
        }
    }
    WAIT_VM(0);
    BARRIER();
    if (tid == 0) { set_flag(rws, WS_F1 + (cb * 16 + tile.rb) * FL, T); set_flag(rws, WS_F0 + blockIdx.x * FL, T); }
    asm volatile(".Lmk_code_end:");
}

__device__ __forceinline__ void phase_ffn1(const Args& a, unsigned lds0, int w, int lane, rsrc_t rws, Tag T, const Code& code) {
    const T1Tile<1> tile;
    const int tid = threadIdx.x, r32 = lane & 31, hi = lane >> 5;
    if (!tile.active) {
        const int idx = (blockIdx.x & 7) * 4 + ((blockIdx.x >> 3) & 3), u = idx * 8 + w;
        const unsigned lw = lds0 + 16384 + w * 10240;
        if (u < 216) retile_issue(a.w2, F_, u / 27, u % 27, lw, lane);
        if (idx < 14) {
            ColSum cs;
            cs.load(a.w1, F_, idx, tid);
            cs.finish(F_, idx, rws, WS_C1, T, lds0, tid);
        }
        if (u < 216) retile_finish(rws, WS_WT2 + u * 10240, lw, lane);
        WAIT_VM(0);
        BARRIER();
        if (tid == 0) set_flag(rws, WS_FW + (16 + idx) * FL, T);
        if (w >= 4) wt_preload<1, 0, T2_NB>(a, lds0, w - 4, lane, rws, T, (u32x2){~T.lo, 0u});
        if (blockIdx.x == 255 && w == 0) {
#pragma unroll 1
            for (int i = 0; i < 4; ++i) wait_flags(rws, WS_F0 + (i * 64 + lane) * FL, T);
            if (lane == 0) set_flag(rws, WS_E, T);
        }
        return;
    }
    if (w >= 4) {
        const T1Loader<1> L(a, lds0, w, lane, tile.rb, tile.cb);
        BARRIER();
        L.run([&](int c) {
        }, []() {});
        BARRIER();
        const u32x2 fw = ld64(rws, T2Loader<1>::fw_off(lane));
        BARRIER();
        wt_preload<1, 0, T2_NB>(a, lds0, w - 4, lane, rws, T, fw);
        return;
    }
    const int m0 = tile.rb * 128, n0 = tile.cb * 128;
    if (w == 1) code_touch(code, lds0, lane);
    if (w == 0) wait_flags(rws, WS_F3 + (tile.rb * 16 + (lane & 15)) * FL, T);
    BARRIER();
    const int wr = w >> 1, wc = w & 1;
    LAS float* smean = (LAS float*)(lds0 + L_AUX);
    LAS float* srstd = smean + 128;
    LAS float* scol = smean + 256;
    LAS float* sbias = smean + 384;
    const int si = 32 * w + r32;
    const int ncol = n0 + si < F_ ? n0 + si : F_ - 1;
    u32x2 part[8];
#pragma unroll
    for (int c = 0; c < 8; ++c) part[c] = ld64(rws, WS_ST + (c * S_ + m0 + si) * 8);
    const u32x4 gran = ld128(rws, WS_C1 + ncol * 16);
    const unsigned short bias = ((const unsigned short*)a.b1)[ncol];
    f32x16 acc[2][2];
    u32x4 unused;
    t1_consume<1>(lds0, w, lane, false, acc, rws, 0u, unused);
    {
        float s1 = 0.f, s2 = 0.f;
#pragma unroll
        for (int c = 0; c < 8; ++c) { s1 += __uint_as_float(part[c][0]); s2 += __uint_as_float(part[c][1]); }
        const float mean = s1 * (1.f / D_);
        const float var = __builtin_fmaxf(s2 * (1.f / D_) - mean * mean, 0.f);
        const float cs = wait_granule(rws, WS_C1 + ncol * 16, T, gran)[0];
        if (hi) { scol[si] = cs; sbias[si] = bf_lo(bias); }
        else    { smean[si] = mean; srstd[si] = __builtin_amdgcn_rsqf(var + LN_EPS); }
    }
    LGKM0_BARRIER();
#pragma unroll
    for (int u = 0; u < 2; ++u) {
        const int nb = n0 + 64 * wc + 32 * u;
        if (nb < F_) {
            f32x4 cs[4], bs[4];
#pragma unroll
            for (int g = 0; g < 4; ++g) {
                cs[g] = *(LAS const f32x4*)(scol + 64 * wc + 32 * u + 8 * g + 4 * hi);
                bs[g] = *(LAS const f32x4*)(sbias + 64 * wc + 32 * u + 8 * g + 4 * hi);
            }
#pragma unroll
            for (int t = 0; t < 2; ++t) {
                const int ml = 64 * wr + 32 * t + r32;
                const float rs = srstd[ml], mr = -smean[ml] * rs;
                const f32x2 rs2 = {rs, rs}, mr2 = {mr, mr};
                const unsigned rt = (m0 + 64 * wr + 32 * t) >> 5;
                u32x2 pk[4];
#pragma unroll
                for (int g = 0; g < 4; ++g) {
                    const f32x2 a0 = {acc[t][u][4 * g], acc[t][u][4 * g + 1]}, a1 = {acc[t][u][4 * g + 2], acc[t][u][4 * g + 3]};
                    f32x2 v0 = fma2(rs2, a0, fma2(mr2, lo2(cs[g]), lo2(bs[g]))), v1 = fma2(rs2, a1, fma2(mr2, hi2(cs[g]), hi2(bs[g])));
                    v0 = __builtin_elementwise_max(v0, (f32x2){0.f, 0.f}); v1 = __builtin_elementwise_max(v1, (f32x2){0.f, 0.f});
                    pk[g] = (u32x2){pk_bf16(v0 * v0), pk_bf16(v1 * v1)};
                }
#pragma unroll
                for (int gp = 0; gp < 2; ++gp) {
                    const auto x = __builtin_amdgcn_permlane32_swap(pk[2 * gp][0], pk[2 * gp + 1][0], false, false);
                    const auto y = __builtin_amdgcn_permlane32_swap(pk[2 * gp][1], pk[2 * gp + 1][1], false, false);
                    st128(rws, WS_H1 + (rt * 108 + (nb >> 4) + gp) * 1024 + lane * 16, (u32x4){x[0], y[0], x[1], y[1]});
                }
            }
        }
    }
    WAIT_VM(0);
    BARRIER();
    if (tid == 0) set_flag(rws, WS_F4 + (tile.rb * 16 + tile.cb) * FL, T);
}

__global__ __launch_bounds__(512, 2) void block_kernel(Args a) {
    __shared__ __attribute__((aligned(16))) char smem[LDS_TOTAL];
    const unsigned lds0 = (unsigned)(uintptr_t)(LAS char*)smem;
    const int lane = threadIdx.x & 63;
    const int w = __builtin_amdgcn_readfirstlane(threadIdx.x >> 6);
    Code code;
    asm volatile("s_getpc_b64 %0\n.Lmk_code_begin:\n\ts_mov_b32 %1, .Lmk_code_end-.Lmk_code_begin" : "=s"(code.begin), "=s"(code.len));
    if (w == 7) code_touch(code, lds0, lane);
    asm volatile("" :: "s"(a.x), "s"(a.wqkv), "s"(a.wo), "s"(a.w1), "s"(a.b1), "s"(a.w2), "s"(a.b2), "s"(a.out), "s"(a.ws));
    const rsrc_t rws = mk_rsrc(a.ws, WS_END);
    Tag T = {0u, 0u};
    phase_qkv(a, lds0, w, lane, rws, T, code);
    phase_attn(a, lds0, w, lane, rws, T, code);
    phase_t2<0>(a, lds0, w, lane, rws, T, code);
    phase_ffn1(a, lds0, w, lane, rws, T, code);
    phase_t2<1>(a, lds0, w, lane, rws, T, code);
}

extern "C" void kernel_launch(void* const* d_in, const int* in_sizes, int n_in, void* d_out, int out_size, void* d_ws,
                              size_t ws_size, hipStream_t stream) {
    (void)in_sizes; (void)n_in; (void)out_size; (void)ws_size;
    char* ws = (char*)d_ws;
    Args a{d_in[0], d_in[1], d_in[2], d_in[4], d_in[5], d_in[6], d_in[7], d_out, ws};
    block_kernel<<<256, 512, 0, stream>>>(a);
}
```
